```python
import torch

def _softmax(x, axis=-1):
    return torch.softmax(x, dim=axis)
def _gelu(x):
    return torch.nn.functional.gelu(x, approximate="tanh")
def _silu(x):
    return torch.nn.functional.silu(x)
def _sigmoid(x):
    return torch.sigmoid(x)
def _layernorm(x, axis=-1, eps=1e-5):
    m = x.mean(dim=axis, keepdim=True)
    v = x.var(dim=axis, keepdim=True, unbiased=False)
    return (x - m) / torch.sqrt(v + eps)
def _scatter_add(tgt, idx, src, axis=0):
    return tgt.clone().index_add_(axis, idx, src)
def _segment_sum(src, seg, n):
    out = torch.zeros((n,) + src.shape[1:], dtype=src.dtype)
    return out.index_add_(0, seg, src)
def _pad(x, pw):
    flat = tuple(v for pair in reversed(pw) for v in pair)
    return torch.nn.functional.pad(x, flat)

kernel_name = 'standalone__gqa_softmax__near_ridge'

def setup_inputs():

    _w = (lambda a: (lambda t: t.to(torch.bfloat16) if t.is_floating_point() else t)(torch.as_tensor(a)))
    B, Hq, Hkv, Sq, Sk, D = 8, 32, 8, 384, 512, 128
    return {
        "q": _w((lambda *s: torch.randn(*s, dtype=torch.bfloat16) * 0.8)(B, Hq, Sq, D)),
        "k": _w((lambda *s: torch.randn(*s, dtype=torch.bfloat16) * 0.8)(B, Hkv, Sk, D)),
        "v": _w((lambda *s: torch.randn(*s, dtype=torch.bfloat16) * 0.05)(B, Hkv, Sk, D)),
    }

def reference(q, k, v):
    B, Hq, Sq, D = q.shape
    Hkv = k.shape[1]
    rep = Hq // Hkv
    k = (lambda x, n, axis: torch.repeat_interleave(x, n, dim=axis))(k, rep, axis=1)
    v = (lambda x, n, axis: torch.repeat_interleave(x, n, dim=axis))(v, rep, axis=1)
    scale = D ** -0.5
    scores = torch.einsum("bhqd,bhkd->bhqk", q, k) * scale
    p = _softmax(scores, axis=-1)
    return torch.einsum("bhqk,bhkd->bhqd", p, v)
```

```cpp
#include <hip/hip_runtime.h>
#include <hip/hip_bf16.h>
constexpr int B=8,HQ=32,HKV=8,SQ=384,SK=512,D=128;
__device__ __forceinline__ float bf2f(unsigned short x){ return __uint_as_float(((unsigned)x)<<16); }
__global__ __launch_bounds__(128) void naive_kernel(const unsigned short* __restrict__ q,const unsigned short* __restrict__ k,const unsigned short* __restrict__ v,__hip_bfloat16* __restrict__ o){
  __shared__ float qs[D]; __shared__ float p[SK]; __shared__ float red[128];
  const int row=blockIdx.x; const int bh=row/SQ; const int b=bh/HQ,h=bh%HQ,kvh=h/(HQ/HKV);
  const int t=threadIdx.x;
  qs[t]=bf2f(q[(size_t)row*D+t]);
  __syncthreads();
  const unsigned short* kb=k+(size_t)(b*HKV+kvh)*SK*D; const unsigned short* vb=v+(size_t)(b*HKV+kvh)*SK*D;
  float mx=-1e30f;
  for(int j=t;j<SK;j+=128){ float s=0; for(int d=0;d<D;++d) s+=qs[d]*bf2f(kb[(size_t)j*D+d]); s*=0.08838834764831845f; p[j]=s; mx=fmaxf(mx,s);}
  red[t]=mx; __syncthreads();
  for(int s=64;s>0;s>>=1){ if(t<s) red[t]=fmaxf(red[t],red[t+s]); __syncthreads(); }
  mx=red[0]; __syncthreads();
  float sum=0; for(int j=t;j<SK;j+=128){ float e=__expf(p[j]-mx); p[j]=e; sum+=e; }
  red[t]=sum; __syncthreads();
  for(int s=64;s>0;s>>=1){ if(t<s) red[t]+=red[t+s]; __syncthreads(); }
  sum=red[0];
  float acc=0; for(int j=0;j<SK;++j) acc+=p[j]*bf2f(vb[(size_t)j*D+t]);
  o[(size_t)row*D+t]=__float2bfloat16(acc/sum);
}
extern "C" void kernel_launch(void* const* d_in,const int* in_sizes,int n_in,void* d_out,int out_size,void* d_ws,size_t ws_size,hipStream_t stream){
  naive_kernel<<<B*HQ*SQ,128,0,stream>>>((const unsigned short*)d_in[0],(const unsigned short*)d_in[1],(const unsigned short*)d_in[2],(__hip_bfloat16*)d_out);
}
```

```cpp
#include <hip/hip_runtime.h>
#include <hip/hip_bf16.h>

namespace {
constexpr int kSq = 384, kSk = 512, kD = 128;

__device__ __forceinline__ float bf2f(unsigned short x) { return __uint_as_float(((unsigned)x) << 16); }
__device__ __forceinline__ float wave_max(float x) {
  for (int o = 32; o > 0; o >>= 1) x = fmaxf(x, __shfl_xor(x, o));
  return x;
}
__device__ __forceinline__ float wave_sum(float x) {
  for (int o = 32; o > 0; o >>= 1) x += __shfl_xor(x, o);
  return x;
}

__device__ __forceinline__ void slow_rows(const unsigned short* __restrict__ q, const unsigned short* __restrict__ k,
                                          const unsigned short* __restrict__ v, unsigned short* __restrict__ o, int wave, int lane) {
  for (int rr = 0; rr < 96; ++rr) {
    const int row = (rr / 48) * 192 + wave * 48 + (rr % 48);
    const uint4* qr = reinterpret_cast<const uint4*>(q + (size_t)row * kD);
    float s[8];
#pragma unroll
    for (int kk = 0; kk < 8; ++kk) {
      const uint4* kr = reinterpret_cast<const uint4*>(k + (size_t)(kk * 64 + lane) * kD);
      float acc = 0.f;
      for (int c = 0; c < 16; ++c) {
        const uint4 a = qr[c], b = kr[c];
        acc = fmaf(__uint_as_float(a.x << 16), __uint_as_float(b.x << 16), acc);
        acc = fmaf(__uint_as_float(a.x & 0xffff0000u), __uint_as_float(b.x & 0xffff0000u), acc);
        acc = fmaf(__uint_as_float(a.y << 16), __uint_as_float(b.y << 16), acc);
        acc = fmaf(__uint_as_float(a.y & 0xffff0000u), __uint_as_float(b.y & 0xffff0000u), acc);
        acc = fmaf(__uint_as_float(a.z << 16), __uint_as_float(b.z << 16), acc);
        acc = fmaf(__uint_as_float(a.z & 0xffff0000u), __uint_as_float(b.z & 0xffff0000u), acc);
        acc = fmaf(__uint_as_float(a.w << 16), __uint_as_float(b.w << 16), acc);
        acc = fmaf(__uint_as_float(a.w & 0xffff0000u), __uint_as_float(b.w & 0xffff0000u), acc);
      }
      s[kk] = acc * 0.08838834764831845f;
    }
    float m = s[0];
#pragma unroll
    for (int kk = 1; kk < 8; ++kk) m = fmaxf(m, s[kk]);
    m = wave_max(m);
    float l = 0.f;
#pragma unroll
    for (int kk = 0; kk < 8; ++kk) { s[kk] = __expf(s[kk] - m); l += s[kk]; }
    l = wave_sum(l);
    float a0 = 0.f, a1 = 0.f;
#pragma unroll
    for (int kk = 0; kk < 8; ++kk) {
      for (int sl = 0; sl < 64; ++sl) {
        const float p = __shfl(s[kk], sl);
        const unsigned vv = reinterpret_cast<const unsigned*>(v + (size_t)(kk * 64 + sl) * kD)[lane];
        a0 = fmaf(p, __uint_as_float(vv << 16), a0);
        a1 = fmaf(p, __uint_as_float(vv & 0xffff0000u), a1);
      }
    }
    const float il = 1.f / l;
    typedef float f32x2_t __attribute__((ext_vector_type(2)));
    typedef __bf16 bf16x2_t __attribute__((ext_vector_type(2)));
    const f32x2_t rf = {a0 * il, a1 * il};
    reinterpret_cast<unsigned*>(o + (size_t)row * kD)[lane] = __builtin_bit_cast(unsigned, __builtin_convertvector(rf, bf16x2_t));
  }
}
}

__global__ __launch_bounds__(256, 1) void gqa_attn_fwd(const unsigned short* __restrict__ q, const unsigned short* __restrict__ k,
                                                       const unsigned short* __restrict__ v, unsigned short* __restrict__ o) {
  __shared__ __attribute__((aligned(1024))) char lds[163840];
  const unsigned bid = blockIdx.x;
  const unsigned kvh = (bid & 7u) * 8u + (bid >> 5);
  const unsigned head = kvh * 4u + ((bid >> 3) & 3u);
  const unsigned short* qh = q + (size_t)head * (kSq * kD);
  const unsigned short* kh = k + (size_t)kvh * (kSk * kD);
  const unsigned short* vh = v + (size_t)kvh * (kSk * kD);
  unsigned short* oh = o + (size_t)head * (kSq * kD);
  unsigned tid = threadIdx.x;
  unsigned long long flag;
  unsigned wid;
  const unsigned ldsb = (unsigned)(size_t)lds;
  asm volatile(
    "Lstart%=:\n"
    "s_getpc_b64 s[84:85]\n"
    "Lpc%=:\n"
    "s_add_u32 s84, s84, Lstart%=-Lpc%=\n"
    "s_addc_u32 s85, s85, -1\n"
    "s_mov_b32 s86, Lend%=-Lstart%=\n"
    "s_mov_b32 s87, 0x20000\n"
    "s_lshr_b32 s79, %[bid], 3\n"
    "s_and_b32 s79, s79, 15\n"
    "s_lshl_b32 s79, s79, 12\n"
    "v_lshlrev_b32 v1, 4, v0\n"
    "buffer_load_dwordx4 v[228:231], v1, s[84:87], s79 offen\n"
    "s_mov_b64 s[40:41], %[k]\n"
    "s_mov_b32 s42, 131072\n"
    "s_mov_b32 s43, 0x20000\n"
    "s_mov_b64 s[48:49], %[q]\n"
    "s_mov_b32 s50, 98304\n"
    "s_mov_b32 s51, 0x20000\n"
    "s_mov_b64 s[44:45], %[v]\n"
    "s_mov_b32 s46, 131072\n"
    "s_mov_b32 s47, 0x20000\n"
    "s_mov_b64 s[52:53], %[o]\n"
    "s_mov_b32 s54, 98304\n"
    "s_mov_b32 s55, 0x20000\n"
    "v_lshrrev_b32 v232, 6, v0\n"
    "s_nop 0\n"
    "v_readfirstlane_b32 s71, v232\n"
    "v_and_b32 v232, 63, v0\n"
    "v_and_b32 v233, 15, v232\n"
    "v_lshrrev_b32 v234, 4, v232\n"
    "v_and_b32 v235, 7, v232\n"
    "v_xor_b32 v235, v235, v234\n"
    "v_lshlrev_b32 v235, 4, v235\n"
    "v_lshrrev_b32 v236, 3, v232\n"
    "v_lshl_add_u32 v5, v236, 8, v235\n"
    "v_xor_b32 v6, 64, v5\n"
    "s_and_b32 s62, s71, 1\n"
    "s_lshl_b32 s62, s62, 13\n"
    "s_lshr_b32 s63, s71, 1\n"
    "s_lshl_b32 s63, s63, 7\n"
    "s_add_u32 s57, s62, s63\n"
    "s_lshl_b32 s59, s71, 12\n"
    "s_add_u32 s58, s59, %[lds]\n"
    "s_mul_i32 s61, s71, 12288\n"
    "s_add_u32 s78, s61, %[lds]\n"
    "s_add_u32 s78, s78, 114688\n"
    "s_add_u32 s62, s57, 0\n"
    "s_add_u32 m0, s58, 0\n"
    "s_nop 0\n"
    "buffer_load_dwordx4 v5, s[40:43], s62 offen nt lds\n"
    "s_add_u32 s63, s57, 2048\n"
    "s_add_u32 m0, s58, 1024\n"
    "s_nop 0\n"
    "buffer_load_dwordx4 v6, s[40:43], s63 offen nt lds\n"
    "s_add_u32 s64, s57, 4096\n"
    "s_add_u32 m0, s58, 2048\n"
    "s_nop 0\n"
    "buffer_load_dwordx4 v5, s[40:43], s64 offen nt lds\n"
    "s_add_u32 s65, s57, 6144\n"
    "s_add_u32 m0, s58, 3072\n"
    "s_nop 0\n"
    "buffer_load_dwordx4 v6, s[40:43], s65 offen nt lds\n"
    "s_add_u32 s62, s61, 0\n"
    "s_add_u32 m0, s78, 0\n"
    "s_nop 0\n"
    "buffer_load_dwordx4 v5, s[48:51], s62 offen nt lds\n"
    "s_add_u32 s63, s61, 2048\n"
    "s_add_u32 m0, s78, 1024\n"
    "s_nop 0\n"
    "buffer_load_dwordx4 v6, s[48:51], s63 offen nt lds\n"
    "s_add_u32 s64, s61, 128\n"
    "s_add_u32 m0, s78, 2048\n"
    "s_nop 0\n"
    "buffer_load_dwordx4 v5, s[48:51], s64 offen nt lds\n"
    "s_add_u32 s65, s61, 2176\n"
    "s_add_u32 m0, s78, 3072\n"
    "s_nop 0\n"
    "buffer_load_dwordx4 v6, s[48:51], s65 offen nt lds\n"
    "s_add_u32 s62, s61, 4096\n"
    "s_add_u32 m0, s78, 4096\n"
    "s_nop 0\n"
    "buffer_load_dwordx4 v5, s[48:51], s62 offen nt lds\n"
    "s_add_u32 s63, s61, 6144\n"
    "s_add_u32 m0, s78, 5120\n"
    "s_nop 0\n"
    "buffer_load_dwordx4 v6, s[48:51], s63 offen nt lds\n"
    "s_add_u32 s64, s61, 4224\n"
    "s_add_u32 m0, s78, 6144\n"
    "s_nop 0\n"
    "buffer_load_dwordx4 v5, s[48:51], s64 offen nt lds\n"
    "s_add_u32 s65, s61, 6272\n"
    "s_add_u32 m0, s78, 7168\n"
    "s_nop 0\n"
    "buffer_load_dwordx4 v6, s[48:51], s65 offen nt lds\n"
    "s_add_u32 s62, s61, 8192\n"
    "s_add_u32 m0, s78, 8192\n"
    "s_nop 0\n"
    "buffer_load_dwordx4 v5, s[48:51], s62 offen nt lds\n"
    "s_add_u32 s63, s61, 10240\n"
    "s_add_u32 m0, s78, 9216\n"
    "s_nop 0\n"
    "buffer_load_dwordx4 v6, s[48:51], s63 offen nt lds\n"
    "s_add_u32 s64, s61, 8320\n"
    "s_add_u32 m0, s78, 10240\n"
    "s_nop 0\n"
    "buffer_load_dwordx4 v5, s[48:51], s64 offen nt lds\n"
    "s_add_u32 s65, s61, 10368\n"
    "s_add_u32 m0, s78, 11264\n"
    "s_nop 0\n"
    "buffer_load_dwordx4 v6, s[48:51], s65 offen nt lds\n"
    "s_add_u32 s60, s58, 65536\n"
    "s_add_u32 s72, s61, 0\n"
    "s_add_u32 s73, s61, 4096\n"
    "s_add_u32 s74, s61, 8192\n"
    "s_add_u32 s75, s61, 49152\n"
    "s_add_u32 s76, s61, 53248\n"
    "s_add_u32 s77, s61, 57344\n"
    "s_mov_b32 s56, 0x3e0293ee\n"
    "s_mov_b32 s70, 0x4f800000\n"
    "s_mov_b64 s[68:69], 0\n"
    "v_and_b32 v235, 1, v234\n"
    "v_lshlrev_b32 v235, 1, v235\n"
    "v_and_b32 v236, 3, v232\n"
    "v_xor_b32 v235, v235, v236\n"
    "v_lshlrev_b32 v235, 4, v235\n"
    "v_lshrrev_b32 v236, 5, v232\n"
    "v_lshl_add_u32 v235, v236, 6, v235\n"
    "v_bfe_u32 v236, v232, 2, 3\n"
    "v_lshl_add_u32 v7, v236, 8, v235\n"
    "v_lshrrev_b32 v235, 1, v233\n"
    "v_xor_b32 v235, v235, v234\n"
    "v_lshlrev_b32 v235, 4, v235\n"
    "v_lshl_add_u32 v235, v233, 7, v235\n"
    "v_add_u32 v1, %[lds], v235\n"
    "v_xor_b32 v2, 64, v1\n"
    "v_add_u32 v8, s78, v235\n"
    "v_xor_b32 v9, 64, v8\n"
    "v_lshrrev_b32 v235, 1, v234\n"
    "v_lshlrev_b32 v235, 11, v235\n"
    "v_and_b32 v236, 1, v234\n"
    "v_mul_u32_u24 v236, 288, v236\n"
    "v_add_u32 v235, v235, v236\n"
    "v_bfe_u32 v236, v232, 2, 2\n"
    "v_lshl_add_u32 v235, v236, 6, v235\n"
    "v_and_b32 v236, 3, v232\n"
    "v_lshl_add_u32 v235, v236, 3, v235\n"
    "v_add_u32 v235, %[lds], v235\n"
    "v_add_u32 v3, 65536, v235\n"
    "v_xor_b32 v4, 32, v3\n"
    "v_lshrrev_b32 v235, 1, v234\n"
    "v_and_b32 v236, 7, v233\n"
    "v_xor_b32 v235, v235, v236\n"
    "v_lshlrev_b32 v235, 4, v235\n"
    "v_lshl_add_u32 v235, v233, 7, v235\n"
    "v_and_b32 v236, 1, v234\n"
    "v_lshl_add_u32 v235, v236, 3, v235\n"
    "v_add_u32 v12, s78, v235\n"
    "v_xor_b32 v13, 32, v12\n"
    "v_xor_b32 v14, 64, v12\n"
    "v_xor_b32 v15, 96, v12\n"
    "v_lshrrev_b32 v236, 3, v232\n"
    "v_and_b32 v235, 7, v232\n"
    "v_lshlrev_b32 v235, 4, v235\n"
    "v_lshl_add_u32 v11, v236, 8, v235\n"
    "v_and_b32 v235, 7, v232\n"
    "v_xor_b32 v235, v235, v236\n"
    "v_lshlrev_b32 v235, 4, v235\n"
    "v_lshl_add_u32 v235, v236, 7, v235\n"
    "v_add_u32 v10, s78, v235\n"
    "v_mov_b32 v235, 0x3f803f80\n"
    "v_accvgpr_write_b32 a236, v235\n"
    "v_accvgpr_write_b32 a237, v235\n"
    "v_accvgpr_write_b32 a238, v235\n"
    "v_accvgpr_write_b32 a239, v235\n"
    "s_waitcnt vmcnt(12)\n"
    "s_barrier\n"
    "ds_read_b128 a[96:99], v1 offset:0\n"
    "ds_read_b128 a[100:103], v2 offset:0\n"
    "ds_read_b128 a[104:107], v1 offset:8192\n"
    "ds_read_b128 a[108:111], v2 offset:8192\n"
    "ds_read_b128 a[112:115], v1 offset:2048\n"
    "ds_read_b128 a[116:119], v2 offset:2048\n"
    "ds_read_b128 a[120:123], v1 offset:10240\n"
    "ds_read_b128 a[124:127], v2 offset:10240\n"
    "ds_read_b128 a[128:131], v1 offset:4096\n"
    "ds_read_b128 a[132:135], v2 offset:4096\n"
    "ds_read_b128 a[136:139], v1 offset:12288\n"
    "ds_read_b128 a[140:143], v2 offset:12288\n"
    "ds_read_b128 a[144:147], v1 offset:6144\n"
    "ds_read_b128 a[148:151], v2 offset:6144\n"
    "ds_read_b128 a[152:155], v1 offset:14336\n"
    "ds_read_b128 a[156:159], v2 offset:14336\n"
    "s_cmp_eq_u32 s71, 0\n"
    "s_cbranch_scc1 Lpg0%=\n"
    "s_cmp_eq_u32 s71, 1\n"
    "s_cbranch_scc1 Lpg1%=\n"
    "s_cmp_eq_u32 s71, 2\n"
    "s_cbranch_scc1 Lpg2%=\n"
    "s_cmp_eq_u32 s71, 3\n"
    "s_cbranch_scc1 Lpg3%=\n"
    "Lback%=:\n"
    "s_cmp_lg_u32 s71, s71\n"
    "s_waitcnt vmcnt(8)\n"
    "ds_read_b128 v[112:115], v8 offset:0\n"
    "ds_read_b128 v[116:119], v9 offset:0\n"
    "ds_read_b128 v[120:123], v8 offset:2048\n"
    "ds_read_b128 v[124:127], v9 offset:2048\n"
    "s_waitcnt lgkmcnt(3)\n"
    "v_lshlrev_b32 v244, 16, v112\n"
    "v_and_b32 v112, 0xffff0000, v112\n"
    "v_mul_f32 v244, s56, v244\n"
    "v_mul_f32 v112, s56, v112\n"
    "v_cvt_pk_bf16_f32 v112, v244, v112\n"
    "v_accvgpr_write_b32 a0, v112\n"
    "v_lshlrev_b32 v244, 16, v113\n"
    "v_and_b32 v113, 0xffff0000, v113\n"
    "v_mul_f32 v244, s56, v244\n"
    "v_mul_f32 v113, s56, v113\n"
    "v_cvt_pk_bf16_f32 v113, v244, v113\n"
    "v_accvgpr_write_b32 a1, v113\n"
    "v_lshlrev_b32 v244, 16, v114\n"
    "v_and_b32 v114, 0xffff0000, v114\n"
    "v_mul_f32 v244, s56, v244\n"
    "v_mul_f32 v114, s56, v114\n"
    "v_cvt_pk_bf16_f32 v114, v244, v114\n"
    "v_accvgpr_write_b32 a2, v114\n"
    "v_lshlrev_b32 v244, 16, v115\n"
    "s_add_u32 s62, s57, 16384\n"
    "s_add_u32 m0, s58, 16384\n"
    "v_and_b32 v115, 0xffff0000, v115\n"
    "v_mul_f32 v244, s56, v244\n"
    "buffer_load_dwordx4 v5, s[40:43], s62 offen nt lds\n"
    "v_mul_f32 v115, s56, v115\n"
    "v_cvt_pk_bf16_f32 v115, v244, v115\n"
    "v_accvgpr_write_b32 a3, v115\n"
    "s_waitcnt lgkmcnt(2)\n"
    "v_lshlrev_b32 v244, 16, v116\n"
    "v_and_b32 v116, 0xffff0000, v116\n"
    "v_mul_f32 v244, s56, v244\n"
    "v_mul_f32 v116, s56, v116\n"
    "v_cvt_pk_bf16_f32 v116, v244, v116\n"
    "v_accvgpr_write_b32 a4, v116\n"
    "v_lshlrev_b32 v244, 16, v117\n"
    "v_and_b32 v117, 0xffff0000, v117\n"
    "v_mul_f32 v244, s56, v244\n"
    "v_mul_f32 v117, s56, v117\n"
    "v_cvt_pk_bf16_f32 v117, v244, v117\n"
    "v_accvgpr_write_b32 a5, v117\n"
    "v_lshlrev_b32 v244, 16, v118\n"
    "v_and_b32 v118, 0xffff0000, v118\n"
    "v_mul_f32 v244, s56, v244\n"
    "v_mul_f32 v118, s56, v118\n"
    "v_cvt_pk_bf16_f32 v118, v244, v118\n"
    "s_add_u32 s63, s57, 18432\n"
    "s_add_u32 m0, s58, 17408\n"
    "v_accvgpr_write_b32 a6, v118\n"
    "v_lshlrev_b32 v244, 16, v119\n"
    "buffer_load_dwordx4 v6, s[40:43], s63 offen nt lds\n"
    "v_and_b32 v119, 0xffff0000, v119\n"
    "v_mul_f32 v244, s56, v244\n"
    "v_mul_f32 v119, s56, v119\n"
    "v_cvt_pk_bf16_f32 v119, v244, v119\n"
    "v_accvgpr_write_b32 a7, v119\n"
    "s_waitcnt lgkmcnt(1)\n"
    "v_lshlrev_b32 v244, 16, v120\n"
    "v_and_b32 v120, 0xffff0000, v120\n"
    "v_mul_f32 v244, s56, v244\n"
    "v_mul_f32 v120, s56, v120\n"
    "v_cvt_pk_bf16_f32 v120, v244, v120\n"
    "v_accvgpr_write_b32 a8, v120\n"
    "v_lshlrev_b32 v244, 16, v121\n"
    "v_and_b32 v121, 0xffff0000, v121\n"
    "v_mul_f32 v244, s56, v244\n"
    "v_mul_f32 v121, s56, v121\n"
    "v_cvt_pk_bf16_f32 v121, v244, v121\n"
    "v_accvgpr_write_b32 a9, v121\n"
    "v_lshlrev_b32 v244, 16, v122\n"
    "v_and_b32 v122, 0xffff0000, v122\n"
    "v_mul_f32 v244, s56, v244\n"
    "s_add_u32 s64, s57, 20480\n"
    "s_add_u32 m0, s58, 18432\n"
    "v_mul_f32 v122, s56, v122\n"
    "v_cvt_pk_bf16_f32 v122, v244, v122\n"
    "buffer_load_dwordx4 v5, s[40:43], s64 offen nt lds\n"
    "v_accvgpr_write_b32 a10, v122\n"
    "v_lshlrev_b32 v244, 16, v123\n"
    "v_and_b32 v123, 0xffff0000, v123\n"
    "v_mul_f32 v244, s56, v244\n"
    "v_mul_f32 v123, s56, v123\n"
    "v_cvt_pk_bf16_f32 v123, v244, v123\n"
    "v_accvgpr_write_b32 a11, v123\n"
    "s_waitcnt lgkmcnt(0)\n"
    "v_lshlrev_b32 v244, 16, v124\n"
    "v_and_b32 v124, 0xffff0000, v124\n"
    "v_mul_f32 v244, s56, v244\n"
    "v_mul_f32 v124, s56, v124\n"
    "v_cvt_pk_bf16_f32 v124, v244, v124\n"
    "v_accvgpr_write_b32 a12, v124\n"
    "v_lshlrev_b32 v244, 16, v125\n"
    "v_and_b32 v125, 0xffff0000, v125\n"
    "v_mul_f32 v244, s56, v244\n"
    "v_mul_f32 v125, s56, v125\n"
    "v_cvt_pk_bf16_f32 v125, v244, v125\n"
    "v_accvgpr_write_b32 a13, v125\n"
    "v_lshlrev_b32 v244, 16, v126\n"
    "s_add_u32 s65, s57, 22528\n"
    "s_add_u32 m0, s58, 19456\n"
    "v_and_b32 v126, 0xffff0000, v126\n"
    "v_mul_f32 v244, s56, v244\n"
    "buffer_load_dwordx4 v6, s[40:43], s65 offen nt lds\n"
    "v_mul_f32 v126, s56, v126\n"
    "v_cvt_pk_bf16_f32 v126, v244, v126\n"
    "v_accvgpr_write_b32 a14, v126\n"
    "v_lshlrev_b32 v244, 16, v127\n"
    "v_and_b32 v127, 0xffff0000, v127\n"
    "v_mul_f32 v244, s56, v244\n"
    "v_mul_f32 v127, s56, v127\n"
    "v_cvt_pk_bf16_f32 v127, v244, v127\n"
    "v_accvgpr_write_b32 a15, v127\n"
    "v_mfma_f32_16x16x32_bf16 v[232:235], a[96:99], a[0:3], 0\n"
    "v_mfma_f32_16x16x32_bf16 v[232:235], a[100:103], a[4:7], v[232:235]\n"
    "v_mfma_f32_16x16x32_bf16 v[232:235], a[104:107], a[8:11], v[232:235]\n"
    "v_mfma_f32_16x16x32_bf16 v[232:235], a[108:111], a[12:15], v[232:235]\n"
    "s_waitcnt vmcnt(8)\n"
    "ds_read_b128 v[128:131], v8 offset:4096\n"
    "ds_read_b128 v[132:135], v9 offset:4096\n"
    "ds_read_b128 v[136:139], v8 offset:6144\n"
    "ds_read_b128 v[140:143], v9 offset:6144\n"
    "s_nop 2\n"
    "v_max3_f32 v220, v232, v233, v234\n"
    "v_max_f32 v220, v220, v235\n"
    "v_mov_b32 v221, v220\n"
    "s_nop 1\n"
    "v_permlane16_swap_b32 v220, v221\n"
    "v_max_f32 v220, v220, v221\n"
    "v_mov_b32 v221, v220\n"
    "s_nop 1\n"
    "v_permlane32_swap_b32 v220, v221\n"
    "v_max_f32 v220, v220, v221\n"
    "v_sub_f32 v208, 0, v220\n"
    "v_mov_b32 v209, v208\n"
    "v_mov_b32 v210, v208\n"
    "v_mov_b32 v211, v208\n"
    "s_waitcnt lgkmcnt(3)\n"
    "v_lshlrev_b32 v244, 16, v128\n"
    "v_and_b32 v128, 0xffff0000, v128\n"
    "v_mul_f32 v244, s56, v244\n"
    "v_mul_f32 v128, s56, v128\n"
    "v_cvt_pk_bf16_f32 v128, v244, v128\n"
    "v_accvgpr_write_b32 a16, v128\n"
    "v_lshlrev_b32 v244, 16, v129\n"
    "v_and_b32 v129, 0xffff0000, v129\n"
    "v_mul_f32 v244, s56, v244\n"
    "v_mul_f32 v129, s56, v129\n"
    "v_cvt_pk_bf16_f32 v129, v244, v129\n"
    "s_add_u32 s62, s59, 0\n"
    "s_add_u32 m0, s60, 0\n"
    "v_accvgpr_write_b32 a17, v129\n"
    "v_lshlrev_b32 v244, 16, v130\n"
    "buffer_load_dwordx4 v7, s[44:47], s62 offen nt lds\n"
    "v_and_b32 v130, 0xffff0000, v130\n"
    "v_mul_f32 v244, s56, v244\n"
    "v_mul_f32 v130, s56, v130\n"
    "v_cvt_pk_bf16_f32 v130, v244, v130\n"
    "v_accvgpr_write_b32 a18, v130\n"
    "v_lshlrev_b32 v244, 16, v131\n"
    "v_and_b32 v131, 0xffff0000, v131\n"
    "v_mul_f32 v244, s56, v244\n"
    "v_mul_f32 v131, s56, v131\n"
    "v_cvt_pk_bf16_f32 v131, v244, v131\n"
    "v_accvgpr_write_b32 a19, v131\n"
    "s_waitcnt lgkmcnt(2)\n"
    "v_lshlrev_b32 v244, 16, v132\n"
    "v_and_b32 v132, 0xffff0000, v132\n"
    "v_mul_f32 v244, s56, v244\n"
    "v_mul_f32 v132, s56, v132\n"
    "v_cvt_pk_bf16_f32 v132, v244, v132\n"
    "v_accvgpr_write_b32 a20, v132\n"
    "v_lshlrev_b32 v244, 16, v133\n"
    "v_and_b32 v133, 0xffff0000, v133\n"
    "v_mul_f32 v244, s56, v244\n"
    "s_add_u32 s63, s59, 128\n"
    "s_add_u32 m0, s60, 1024\n"
    "v_mul_f32 v133, s56, v133\n"
    "v_cvt_pk_bf16_f32 v133, v244, v133\n"
    "buffer_load_dwordx4 v7, s[44:47], s63 offen nt lds\n"
    "v_accvgpr_write_b32 a21, v133\n"
    "v_lshlrev_b32 v244, 16, v134\n"
    "v_and_b32 v134, 0xffff0000, v134\n"
    "v_mul_f32 v244, s56, v244\n"
    "v_mul_f32 v134, s56, v134\n"
    "v_cvt_pk_bf16_f32 v134, v244, v134\n"
    "v_accvgpr_write_b32 a22, v134\n"
    "v_lshlrev_b32 v244, 16, v135\n"
    "v_and_b32 v135, 0xffff0000, v135\n"
    "v_mul_f32 v244, s56, v244\n"
    "v_mul_f32 v135, s56, v135\n"
    "v_cvt_pk_bf16_f32 v135, v244, v135\n"
    "v_accvgpr_write_b32 a23, v135\n"
    "s_waitcnt lgkmcnt(1)\n"
    "v_lshlrev_b32 v244, 16, v136\n"
    "v_and_b32 v136, 0xffff0000, v136\n"
    "v_mul_f32 v244, s56, v244\n"
    "v_mul_f32 v136, s56, v136\n"
    "v_cvt_pk_bf16_f32 v136, v244, v136\n"
    "v_accvgpr_write_b32 a24, v136\n"
    "v_lshlrev_b32 v244, 16, v137\n"
    "s_add_u32 s64, s59, 2048\n"
    "s_add_u32 m0, s60, 2048\n"
    "v_and_b32 v137, 0xffff0000, v137\n"
    "v_mul_f32 v244, s56, v244\n"
    "buffer_load_dwordx4 v7, s[44:47], s64 offen nt lds\n"
    "v_mul_f32 v137, s56, v137\n"
    "v_cvt_pk_bf16_f32 v137, v244, v137\n"
    "v_accvgpr_write_b32 a25, v137\n"
    "v_lshlrev_b32 v244, 16, v138\n"
    "v_and_b32 v138, 0xffff0000, v138\n"
    "v_mul_f32 v244, s56, v244\n"
    "v_mul_f32 v138, s56, v138\n"
    "v_cvt_pk_bf16_f32 v138, v244, v138\n"
    "v_accvgpr_write_b32 a26, v138\n"
    "v_lshlrev_b32 v244, 16, v139\n"
    "v_and_b32 v139, 0xffff0000, v139\n"
    "v_mul_f32 v244, s56, v244\n"
    "v_mul_f32 v139, s56, v139\n"
    "v_cvt_pk_bf16_f32 v139, v244, v139\n"
    "v_accvgpr_write_b32 a27, v139\n"
    "s_waitcnt lgkmcnt(0)\n"
    "v_lshlrev_b32 v244, 16, v140\n"
    "v_and_b32 v140, 0xffff0000, v140\n"
    "v_mul_f32 v244, s56, v244\n"
    "v_mul_f32 v140, s56, v140\n"
    "v_cvt_pk_bf16_f32 v140, v244, v140\n"
    "s_add_u32 s65, s59, 2176\n"
    "s_add_u32 m0, s60, 3072\n"
    "v_accvgpr_write_b32 a28, v140\n"
    "v_lshlrev_b32 v244, 16, v141\n"
    "buffer_load_dwordx4 v7, s[44:47], s65 offen nt lds\n"
    "v_and_b32 v141, 0xffff0000, v141\n"
    "v_mul_f32 v244, s56, v244\n"
    "v_mul_f32 v141, s56, v141\n"
    "v_cvt_pk_bf16_f32 v141, v244, v141\n"
    "v_accvgpr_write_b32 a29, v141\n"
    "v_lshlrev_b32 v244, 16, v142\n"
    "v_and_b32 v142, 0xffff0000, v142\n"
    "v_mul_f32 v244, s56, v244\n"
    "v_mul_f32 v142, s56, v142\n"
    "v_cvt_pk_bf16_f32 v142, v244, v142\n"
    "v_accvgpr_write_b32 a30, v142\n"
    "v_lshlrev_b32 v244, 16, v143\n"
    "v_and_b32 v143, 0xffff0000, v143\n"
    "v_mul_f32 v244, s56, v244\n"
    "v_mul_f32 v143, s56, v143\n"
    "v_cvt_pk_bf16_f32 v143, v244, v143\n"
    "v_accvgpr_write_b32 a31, v143\n"
    "v_mfma_f32_16x16x32_bf16 v[236:239], a[96:99], a[16:19], 0\n"
    "v_mfma_f32_16x16x32_bf16 v[236:239], a[100:103], a[20:23], v[236:239]\n"
    "v_mfma_f32_16x16x32_bf16 v[236:239], a[104:107], a[24:27], v[236:239]\n"
    "v_mfma_f32_16x16x32_bf16 v[236:239], a[108:111], a[28:31], v[236:239]\n"
    "s_waitcnt vmcnt(8)\n"
    "ds_read_b128 v[144:147], v8 offset:8192\n"
    "ds_read_b128 v[148:151], v9 offset:8192\n"
    "ds_read_b128 v[152:155], v8 offset:10240\n"
    "ds_read_b128 v[156:159], v9 offset:10240\n"
    "s_nop 2\n"
    "v_max3_f32 v224, v236, v237, v238\n"
    "v_max_f32 v224, v224, v239\n"
    "v_mov_b32 v225, v224\n"
    "s_nop 1\n"
    "v_permlane16_swap_b32 v224, v225\n"
    "v_max_f32 v224, v224, v225\n"
    "v_mov_b32 v225, v224\n"
    "s_nop 1\n"
    "v_permlane32_swap_b32 v224, v225\n"
    "v_max_f32 v224, v224, v225\n"
    "v_sub_f32 v212, 0, v224\n"
    "v_mov_b32 v213, v212\n"
    "v_mov_b32 v214, v212\n"
    "v_mov_b32 v215, v212\n"
    "s_waitcnt lgkmcnt(3)\n"
    "v_lshlrev_b32 v244, 16, v144\n"
    "v_and_b32 v144, 0xffff0000, v144\n"
    "v_mul_f32 v244, s56, v244\n"
    "s_add_u32 s62, s57, 32768\n"
    "s_add_u32 m0, s58, 32768\n"
    "v_mul_f32 v144, s56, v144\n"
    "v_cvt_pk_bf16_f32 v144, v244, v144\n"
    "buffer_load_dwordx4 v5, s[40:43], s62 offen nt lds\n"
    "v_accvgpr_write_b32 a32, v144\n"
    "v_lshlrev_b32 v244, 16, v145\n"
    "v_and_b32 v145, 0xffff0000, v145\n"
    "v_mul_f32 v244, s56, v244\n"
    "v_mul_f32 v145, s56, v145\n"
    "v_cvt_pk_bf16_f32 v145, v244, v145\n"
    "v_accvgpr_write_b32 a33, v145\n"
    "v_lshlrev_b32 v244, 16, v146\n"
    "v_and_b32 v146, 0xffff0000, v146\n"
    "v_mul_f32 v244, s56, v244\n"
    "v_mul_f32 v146, s56, v146\n"
    "v_cvt_pk_bf16_f32 v146, v244, v146\n"
    "v_accvgpr_write_b32 a34, v146\n"
    "v_lshlrev_b32 v244, 16, v147\n"
    "v_and_b32 v147, 0xffff0000, v147\n"
    "v_mul_f32 v244, s56, v244\n"
    "v_mul_f32 v147, s56, v147\n"
    "v_cvt_pk_bf16_f32 v147, v244, v147\n"
    "v_accvgpr_write_b32 a35, v147\n"
    "s_waitcnt lgkmcnt(2)\n"
    "v_lshlrev_b32 v244, 16, v148\n"
    "s_add_u32 s63, s57, 34816\n"
    "s_add_u32 m0, s58, 33792\n"
    "v_and_b32 v148, 0xffff0000, v148\n"
    "v_mul_f32 v244, s56, v244\n"
    "buffer_load_dwordx4 v6, s[40:43], s63 offen nt lds\n"
    "v_mul_f32 v148, s56, v148\n"
    "v_cvt_pk_bf16_f32 v148, v244, v148\n"
    "v_accvgpr_write_b32 a36, v148\n"
    "v_lshlrev_b32 v244, 16, v149\n"
    "v_and_b32 v149, 0xffff0000, v149\n"
    "v_mul_f32 v244, s56, v244\n"
    "v_mul_f32 v149, s56, v149\n"
    "v_cvt_pk_bf16_f32 v149, v244, v149\n"
    "v_accvgpr_write_b32 a37, v149\n"
    "v_lshlrev_b32 v244, 16, v150\n"
    "v_and_b32 v150, 0xffff0000, v150\n"
    "v_mul_f32 v244, s56, v244\n"
    "v_mul_f32 v150, s56, v150\n"
    "v_cvt_pk_bf16_f32 v150, v244, v150\n"
    "v_accvgpr_write_b32 a38, v150\n"
    "v_lshlrev_b32 v244, 16, v151\n"
    "v_and_b32 v151, 0xffff0000, v151\n"
    "v_mul_f32 v244, s56, v244\n"
    "v_mul_f32 v151, s56, v151\n"
    "v_cvt_pk_bf16_f32 v151, v244, v151\n"
    "s_add_u32 s64, s57, 36864\n"
    "s_add_u32 m0, s58, 34816\n"
    "v_accvgpr_write_b32 a39, v151\n"
    "s_waitcnt lgkmcnt(1)\n"
    "v_lshlrev_b32 v244, 16, v152\n"
    "buffer_load_dwordx4 v5, s[40:43], s64 offen nt lds\n"
    "v_and_b32 v152, 0xffff0000, v152\n"
    "v_mul_f32 v244, s56, v244\n"
    "v_mul_f32 v152, s56, v152\n"
    "v_cvt_pk_bf16_f32 v152, v244, v152\n"
    "v_accvgpr_write_b32 a40, v152\n"
    "v_lshlrev_b32 v244, 16, v153\n"
    "v_and_b32 v153, 0xffff0000, v153\n"
    "v_mul_f32 v244, s56, v244\n"
    "v_mul_f32 v153, s56, v153\n"
    "v_cvt_pk_bf16_f32 v153, v244, v153\n"
    "v_accvgpr_write_b32 a41, v153\n"
    "v_lshlrev_b32 v244, 16, v154\n"
    "v_and_b32 v154, 0xffff0000, v154\n"
    "v_mul_f32 v244, s56, v244\n"
    "v_mul_f32 v154, s56, v154\n"
    "v_cvt_pk_bf16_f32 v154, v244, v154\n"
    "v_accvgpr_write_b32 a42, v154\n"
    "v_lshlrev_b32 v244, 16, v155\n"
    "v_and_b32 v155, 0xffff0000, v155\n"
    "v_mul_f32 v244, s56, v244\n"
    "s_add_u32 s65, s57, 38912\n"
    "s_add_u32 m0, s58, 35840\n"
    "v_mul_f32 v155, s56, v155\n"
    "v_cvt_pk_bf16_f32 v155, v244, v155\n"
    "buffer_load_dwordx4 v6, s[40:43], s65 offen nt lds\n"
    "v_accvgpr_write_b32 a43, v155\n"
    "s_waitcnt lgkmcnt(0)\n"
    "v_lshlrev_b32 v244, 16, v156\n"
    "v_and_b32 v156, 0xffff0000, v156\n"
    "v_mul_f32 v244, s56, v244\n"
    "v_mul_f32 v156, s56, v156\n"
    "v_cvt_pk_bf16_f32 v156, v244, v156\n"
    "v_accvgpr_write_b32 a44, v156\n"
    "v_lshlrev_b32 v244, 16, v157\n"
    "v_and_b32 v157, 0xffff0000, v157\n"
    "v_mul_f32 v244, s56, v244\n"
    "v_mul_f32 v157, s56, v157\n"
    "v_cvt_pk_bf16_f32 v157, v244, v157\n"
    "v_accvgpr_write_b32 a45, v157\n"
    "v_lshlrev_b32 v244, 16, v158\n"
    "v_and_b32 v158, 0xffff0000, v158\n"
    "v_mul_f32 v244, s56, v244\n"
    "v_mul_f32 v158, s56, v158\n"
    "v_cvt_pk_bf16_f32 v158, v244, v158\n"
    "v_accvgpr_write_b32 a46, v158\n"
    "v_lshlrev_b32 v244, 16, v159\n"
    "v_and_b32 v159, 0xffff0000, v159\n"
    "v_mul_f32 v244, s56, v244\n"
    "v_mul_f32 v159, s56, v159\n"
    "v_cvt_pk_bf16_f32 v159, v244, v159\n"
    "v_accvgpr_write_b32 a47, v159\n"
    "v_mfma_f32_16x16x32_bf16 v[240:243], a[96:99], a[32:35], 0\n"
    "v_mfma_f32_16x16x32_bf16 v[240:243], a[100:103], a[36:39], v[240:243]\n"
    "v_mfma_f32_16x16x32_bf16 v[240:243], a[104:107], a[40:43], v[240:243]\n"
    "v_mfma_f32_16x16x32_bf16 v[240:243], a[108:111], a[44:47], v[240:243]\n"
    "s_nop 7\n"
    "v_max3_f32 v228, v240, v241, v242\n"
    "v_max_f32 v228, v228, v243\n"
    "v_mov_b32 v229, v228\n"
    "s_nop 1\n"
    "v_permlane16_swap_b32 v228, v229\n"
    "v_max_f32 v228, v228, v229\n"
    "v_mov_b32 v229, v228\n"
    "s_nop 1\n"
    "v_permlane32_swap_b32 v228, v229\n"
    "v_max_f32 v228, v228, v229\n"
    "v_sub_f32 v216, 0, v228\n"
    "v_mov_b32 v217, v216\n"
    "v_mov_b32 v218, v216\n"
    "v_mov_b32 v219, v216\n"
    "s_waitcnt vmcnt(8) lgkmcnt(0)\n"
    "s_barrier\n"
    "s_branch Lmain%=\n"
    ".p2align 12\n"
    "Lmain%=:\n"
    "v_mfma_f32_16x16x32_bf16 v[112:115], a[96:99], a[0:3], v[208:211]\n"
    "Lpg0%=:\n"
    "s_cbranch_scc1 Lpg4%=\n"
    "v_mfma_f32_16x16x32_bf16 v[112:115], a[100:103], a[4:7], v[112:115]\n"
    "s_add_u32 s62, s57, 49152\n"
    "v_mfma_f32_16x16x32_bf16 v[112:115], a[104:107], a[8:11], v[112:115]\n"
    "s_add_u32 m0, s58, 49152\n"
    "v_mfma_f32_16x16x32_bf16 v[112:115], a[108:111], a[12:15], v[112:115]\n"
    "buffer_load_dwordx4 v5, s[40:43], s62 offen nt lds\n"
    "v_mfma_f32_16x16x32_bf16 v[128:131], a[96:99], a[16:19], v[212:215]\n"
    "s_add_u32 s63, s57, 51200\n"
    "v_mfma_f32_16x16x32_bf16 v[128:131], a[100:103], a[20:23], v[128:131]\n"
    "s_add_u32 m0, s58, 50176\n"
    "v_mfma_f32_16x16x32_bf16 v[128:131], a[104:107], a[24:27], v[128:131]\n"
    "buffer_load_dwordx4 v6, s[40:43], s63 offen nt lds\n"
    "v_mfma_f32_16x16x32_bf16 v[128:131], a[108:111], a[28:31], v[128:131]\n"
    "s_add_u32 s64, s57, 53248\n"
    "v_mfma_f32_16x16x32_bf16 v[144:147], a[96:99], a[32:35], v[216:219]\n"
    "s_add_u32 m0, s58, 51200\n"
    "v_mfma_f32_16x16x32_bf16 v[144:147], a[100:103], a[36:39], v[144:147]\n"
    "buffer_load_dwordx4 v5, s[40:43], s64 offen nt lds\n"
    "v_mfma_f32_16x16x32_bf16 v[144:147], a[104:107], a[40:43], v[144:147]\n"
    "ds_read_b128 a[96:99], v1 offset:16384\n"
    "v_mfma_f32_16x16x32_bf16 v[144:147], a[108:111], a[44:47], v[144:147]\n"
    "s_add_u32 s65, s57, 55296\n"
    "v_mfma_f32_16x16x32_bf16 v[116:119], a[112:115], a[0:3], v[208:211]\n"
    "s_add_u32 m0, s58, 52224\n"
    "ds_read_b128 a[100:103], v2 offset:16384\n"
    "v_mfma_f32_16x16x32_bf16 v[116:119], a[116:119], a[4:7], v[116:119]\n"
    "buffer_load_dwordx4 v6, s[40:43], s65 offen nt lds\n"
    "v_mfma_f32_16x16x32_bf16 v[116:119], a[120:123], a[8:11], v[116:119]\n"
    "s_add_u32 s62, s57, 65536\n"
    "ds_read_b128 a[104:107], v1 offset:24576\n"
    "v_mfma_f32_16x16x32_bf16 v[116:119], a[124:127], a[12:15], v[116:119]\n"
    "s_add_u32 m0, s58, 0\n"
    "v_mfma_f32_16x16x32_bf16 v[132:135], a[112:115], a[16:19], v[212:215]\n"
    "buffer_load_dwordx4 v5, s[40:43], s62 offen nt lds\n"
    "v_mfma_f32_16x16x32_bf16 v[132:135], a[116:119], a[20:23], v[132:135]\n"
    "s_add_u32 s63, s57, 67584\n"
    "ds_read_b128 a[108:111], v2 offset:24576\n"
    "v_mfma_f32_16x16x32_bf16 v[132:135], a[120:123], a[24:27], v[132:135]\n"
    "s_add_u32 m0, s58, 1024\n"
    "v_mfma_f32_16x16x32_bf16 v[132:135], a[124:127], a[28:31], v[132:135]\n"
    "buffer_load_dwordx4 v6, s[40:43], s63 offen nt lds\n"
    "v_mfma_f32_16x16x32_bf16 v[148:151], a[112:115], a[32:35], v[216:219]\n"
    "v_mfma_f32_16x16x32_bf16 v[148:151], a[116:119], a[36:39], v[148:151]\n"
    "s_add_u32 s64, s57, 69632\n"
    "ds_read_b128 a[112:115], v1 offset:18432\n"
    "v_mfma_f32_16x16x32_bf16 v[148:151], a[120:123], a[40:43], v[148:151]\n"
    "s_add_u32 m0, s58, 2048\n"
    "v_mfma_f32_16x16x32_bf16 v[148:151], a[124:127], a[44:47], v[148:151]\n"
    "buffer_load_dwordx4 v5, s[40:43], s64 offen nt lds\n"
    "v_mfma_f32_16x16x32_bf16 v[120:123], a[128:131], a[0:3], v[208:211]\n"
    "s_add_u32 s65, s57, 71680\n"
    "ds_read_b128 a[116:119], v2 offset:18432\n"
    "v_mfma_f32_16x16x32_bf16 v[120:123], a[132:135], a[4:7], v[120:123]\n"
    "s_add_u32 m0, s58, 3072\n"
    "v_mfma_f32_16x16x32_bf16 v[120:123], a[136:139], a[8:11], v[120:123]\n"
    "buffer_load_dwordx4 v6, s[40:43], s65 offen nt lds\n"
    "v_mfma_f32_16x16x32_bf16 v[120:123], a[140:143], a[12:15], v[120:123]\n"
    "s_add_u32 s62, s59, 16384\n"
    "ds_read_b128 a[120:123], v1 offset:26624\n"
    "v_mfma_f32_16x16x32_bf16 v[136:139], a[128:131], a[16:19], v[212:215]\n"
    "s_add_u32 m0, s60, 16384\n"
    "v_mfma_f32_16x16x32_bf16 v[136:139], a[132:135], a[20:23], v[136:139]\n"
    "buffer_load_dwordx4 v7, s[44:47], s62 offen nt lds\n"
    "v_mfma_f32_16x16x32_bf16 v[136:139], a[136:139], a[24:27], v[136:139]\n"
    "ds_read_b128 a[124:127], v2 offset:26624\n"
    "v_mfma_f32_16x16x32_bf16 v[136:139], a[140:143], a[28:31], v[136:139]\n"
    "s_add_u32 s63, s59, 16512\n"
    "v_mfma_f32_16x16x32_bf16 v[152:155], a[128:131], a[32:35], v[216:219]\n"
    "s_add_u32 m0, s60, 17408\n"
    "v_mfma_f32_16x16x32_bf16 v[152:155], a[132:135], a[36:39], v[152:155]\n"
    "s_add_u32 s64, s59, 18432\n"
    "buffer_load_dwordx4 v7, s[44:47], s63 offen nt lds\n"
    "v_mfma_f32_16x16x32_bf16 v[152:155], a[136:139], a[40:43], v[152:155]\n"
    "s_add_u32 m0, s60, 18432\n"
    "ds_read_b128 a[128:131], v1 offset:20480\n"
    "v_mfma_f32_16x16x32_bf16 v[152:155], a[140:143], a[44:47], v[152:155]\n"
    "buffer_load_dwordx4 v7, s[44:47], s64 offen nt lds\n"
    "v_mfma_f32_16x16x32_bf16 v[124:127], a[144:147], a[0:3], v[208:211]\n"
    "v_exp_f32 v112, v112\n"
    "v_mfma_f32_16x16x32_bf16 v[124:127], a[148:151], a[4:7], v[124:127]\n"
    "s_add_u32 s65, s59, 18560\n"
    "v_exp_f32 v113, v113\n"
    "v_mfma_f32_16x16x32_bf16 v[124:127], a[152:155], a[8:11], v[124:127]\n"
    "s_add_u32 m0, s60, 19456\n"
    "v_exp_f32 v114, v114\n"
    "v_cvt_pk_bf16_f32 v160, v112, v113\n"
    "v_mfma_f32_16x16x32_bf16 v[124:127], a[156:159], a[12:15], v[124:127]\n"
    "buffer_load_dwordx4 v7, s[44:47], s65 offen nt lds\n"
    "v_exp_f32 v115, v115\n"
    "v_mfma_f32_16x16x32_bf16 v[140:143], a[144:147], a[16:19], v[212:215]\n"
    "ds_read_b128 a[132:135], v2 offset:20480\n"
    "v_exp_f32 v128, v128\n"
    "v_cvt_pk_bf16_f32 v161, v114, v115\n"
    "v_mfma_f32_16x16x32_bf16 v[140:143], a[148:151], a[20:23], v[140:143]\n"
    "v_exp_f32 v129, v129\n"
    "v_mfma_f32_16x16x32_bf16 v[140:143], a[152:155], a[24:27], v[140:143]\n"
    "v_exp_f32 v130, v130\n"
    "v_cvt_pk_bf16_f32 v176, v128, v129\n"
    "v_mfma_f32_16x16x32_bf16 v[140:143], a[156:159], a[28:31], v[140:143]\n"
    "ds_read_b128 a[136:139], v1 offset:28672\n"
    "v_exp_f32 v131, v131\n"
    "v_mfma_f32_16x16x32_bf16 v[156:159], a[144:147], a[32:35], v[216:219]\n"
    "v_exp_f32 v144, v144\n"
    "v_cvt_pk_bf16_f32 v177, v130, v131\n"
    "v_mfma_f32_16x16x32_bf16 v[156:159], a[148:151], a[36:39], v[156:159]\n"
    "v_exp_f32 v145, v145\n"
    "v_mfma_f32_16x16x32_bf16 v[156:159], a[152:155], a[40:43], v[156:159]\n"
    "s_waitcnt vmcnt(12) lgkmcnt(0)\n"
    "ds_read_b128 a[140:143], v2 offset:28672\n"
    "v_exp_f32 v146, v146\n"
    "v_cvt_pk_bf16_f32 v192, v144, v145\n"
    "v_mfma_f32_16x16x32_bf16 v[156:159], a[156:159], a[44:47], v[156:159]\n"
    "s_barrier\n"
    "v_exp_f32 v147, v147\n"
    "v_mfma_f32_16x16x32_bf16 v[112:115], a[96:99], a[0:3], v[208:211]\n"
    "v_exp_f32 v116, v116\n"
    "v_cvt_pk_bf16_f32 v193, v146, v147\n"
    "v_mfma_f32_16x16x32_bf16 v[112:115], a[100:103], a[4:7], v[112:115]\n"
    "s_add_u32 s62, s59, 32768\n"
    "v_exp_f32 v117, v117\n"
    "v_mfma_f32_16x16x32_bf16 v[112:115], a[104:107], a[8:11], v[112:115]\n"
    "s_add_u32 m0, s60, 32768\n"
    "v_exp_f32 v118, v118\n"
    "v_cvt_pk_bf16_f32 v162, v116, v117\n"
    "v_mfma_f32_16x16x32_bf16 v[112:115], a[108:111], a[12:15], v[112:115]\n"
    "buffer_load_dwordx4 v7, s[44:47], s62 offen nt lds\n"
    "v_exp_f32 v119, v119\n"
    "v_mfma_f32_16x16x32_bf16 v[128:131], a[96:99], a[16:19], v[212:215]\n"
    "v_exp_f32 v132, v132\n"
    "v_cvt_pk_bf16_f32 v163, v118, v119\n"
    "v_mfma_f32_16x16x32_bf16 v[128:131], a[100:103], a[20:23], v[128:131]\n"
    "v_exp_f32 v133, v133\n"
    "v_mfma_f32_16x16x32_bf16 v[128:131], a[104:107], a[24:27], v[128:131]\n"
    "v_exp_f32 v134, v134\n"
    "v_cvt_pk_bf16_f32 v178, v132, v133\n"
    "v_mfma_f32_16x16x32_bf16 v[128:131], a[108:111], a[28:31], v[128:131]\n"
    "v_exp_f32 v135, v135\n"
    "v_mfma_f32_16x16x32_bf16 v[144:147], a[96:99], a[32:35], v[216:219]\n"
    "v_exp_f32 v148, v148\n"
    "v_cvt_pk_bf16_f32 v179, v134, v135\n"
    "v_mfma_f32_16x16x32_bf16 v[144:147], a[100:103], a[36:39], v[144:147]\n"
    "ds_read_b128 a[144:147], v1 offset:22528\n"
    "v_exp_f32 v149, v149\n"
    "v_mfma_f32_16x16x32_bf16 v[144:147], a[104:107], a[40:43], v[144:147]\n"
    "v_exp_f32 v150, v150\n"
    "v_cvt_pk_bf16_f32 v194, v148, v149\n"
    "v_mfma_f32_16x16x32_bf16 v[144:147], a[108:111], a[44:47], v[144:147]\n"
    "v_exp_f32 v151, v151\n"
    "v_mfma_f32_16x16x32_bf16 v[116:119], a[112:115], a[0:3], v[208:211]\n"
    "ds_read_b128 a[148:151], v2 offset:22528\n"
    "v_exp_f32 v120, v120\n"
    "v_cvt_pk_bf16_f32 v195, v150, v151\n"
    "v_mfma_f32_16x16x32_bf16 v[116:119], a[116:119], a[4:7], v[116:119]\n"
    "v_exp_f32 v121, v121\n"
    "v_mfma_f32_16x16x32_bf16 v[116:119], a[120:123], a[8:11], v[116:119]\n"
    "v_exp_f32 v122, v122\n"
    "v_cvt_pk_bf16_f32 v164, v120, v121\n"
    "v_mfma_f32_16x16x32_bf16 v[116:119], a[124:127], a[12:15], v[116:119]\n"
    "ds_read_b128 a[152:155], v1 offset:30720\n"
    "v_exp_f32 v123, v123\n"
    "v_mfma_f32_16x16x32_bf16 v[132:135], a[112:115], a[16:19], v[212:215]\n"
    "v_exp_f32 v136, v136\n"
    "v_cvt_pk_bf16_f32 v165, v122, v123\n"
    "v_mfma_f32_16x16x32_bf16 v[132:135], a[116:119], a[20:23], v[132:135]\n"
    "v_exp_f32 v137, v137\n"
    "v_mfma_f32_16x16x32_bf16 v[132:135], a[120:123], a[24:27], v[132:135]\n"
    "ds_read_b128 a[156:159], v2 offset:30720\n"
    "v_exp_f32 v138, v138\n"
    "v_cvt_pk_bf16_f32 v180, v136, v137\n"
    "v_mfma_f32_16x16x32_bf16 v[132:135], a[124:127], a[28:31], v[132:135]\n"
    "v_exp_f32 v139, v139\n"
    "v_mfma_f32_16x16x32_bf16 v[148:151], a[112:115], a[32:35], v[216:219]\n"
    "v_exp_f32 v152, v152\n"
    "v_cvt_pk_bf16_f32 v181, v138, v139\n"
    "v_mfma_f32_16x16x32_bf16 v[148:151], a[116:119], a[36:39], v[148:151]\n"
    "v_exp_f32 v153, v153\n"
    "v_mfma_f32_16x16x32_bf16 v[148:151], a[120:123], a[40:43], v[148:151]\n"
    "v_exp_f32 v154, v154\n"
    "v_cvt_pk_bf16_f32 v196, v152, v153\n"
    "v_mfma_f32_16x16x32_bf16 v[148:151], a[124:127], a[44:47], v[148:151]\n"
    "v_exp_f32 v155, v155\n"
    "v_mfma_f32_16x16x32_bf16 v[120:123], a[128:131], a[0:3], v[208:211]\n"
    "s_add_u32 s63, s59, 32896\n"
    "v_exp_f32 v124, v124\n"
    "v_cvt_pk_bf16_f32 v197, v154, v155\n"
    "v_mfma_f32_16x16x32_bf16 v[120:123], a[132:135], a[4:7], v[120:123]\n"
    "s_add_u32 m0, s60, 33792\n"
    "v_exp_f32 v125, v125\n"
    "v_mfma_f32_16x16x32_bf16 v[120:123], a[136:139], a[8:11], v[120:123]\n"
    "s_waitcnt lgkmcnt(0)\n"
    "buffer_load_dwordx4 v7, s[44:47], s63 offen nt lds\n"
    "v_exp_f32 v126, v126\n"
    "v_mfma_f32_16x16x32_bf16 v[120:123], a[140:143], a[12:15], v[120:123]\n"
    "v_exp_f32 v127, v127\n"
    "v_cvt_pk_bf16_f32 v166, v124, v125\n"
    "v_mfma_f32_16x16x32_bf16 v[136:139], a[128:131], a[16:19], v[212:215]\n"
    "v_exp_f32 v140, v140\n"
    "v_cvt_pk_bf16_f32 v167, v126, v127\n"
    "v_mfma_f32_16x16x32_bf16 v[136:139], a[132:135], a[20:23], v[136:139]\n"
    "v_exp_f32 v141, v141\n"
    "v_mfma_f32_16x16x32_bf16 v[136:139], a[136:139], a[24:27], v[136:139]\n"
    "v_exp_f32 v142, v142\n"
    "v_cvt_pk_bf16_f32 v182, v140, v141\n"
    "v_mfma_f32_16x16x32_bf16 v[136:139], a[140:143], a[28:31], v[136:139]\n"
    "v_exp_f32 v143, v143\n"
    "v_mfma_f32_16x16x32_bf16 v[152:155], a[128:131], a[32:35], v[216:219]\n"
    "v_exp_f32 v156, v156\n"
    "v_cvt_pk_bf16_f32 v183, v142, v143\n"
    "v_mfma_f32_16x16x32_bf16 v[152:155], a[132:135], a[36:39], v[152:155]\n"
    "v_exp_f32 v157, v157\n"
    "v_mfma_f32_16x16x32_bf16 v[152:155], a[136:139], a[40:43], v[152:155]\n"
    "v_exp_f32 v158, v158\n"
    "v_cvt_pk_bf16_f32 v198, v156, v157\n"
    "v_mfma_f32_16x16x32_bf16 v[152:155], a[140:143], a[44:47], v[152:155]\n"
    "v_exp_f32 v159, v159\n"
    "v_mfma_f32_16x16x32_bf16 v[124:127], a[144:147], a[0:3], v[208:211]\n"
    "v_exp_f32 v112, v112\n"
    "v_cvt_pk_bf16_f32 v199, v158, v159\n"
    "v_mfma_f32_16x16x32_bf16 v[124:127], a[148:151], a[4:7], v[124:127]\n"
    "v_exp_f32 v113, v113\n"
    "v_mfma_f32_16x16x32_bf16 v[124:127], a[152:155], a[8:11], v[124:127]\n"
    "v_exp_f32 v114, v114\n"
    "v_mfma_f32_16x16x32_bf16 v[124:127], a[156:159], a[12:15], v[124:127]\n"
    "ds_read_b64_tr_b16 a[160:161], v3 offset:0\n"
    "ds_read_b64_tr_b16 a[162:163], v3 offset:4096\n"
    "v_mfma_f32_16x16x32_bf16 v[140:143], a[144:147], a[16:19], v[212:215]\n"
    "v_exp_f32 v115, v115\n"
    "v_mfma_f32_16x16x32_bf16 v[140:143], a[148:151], a[20:23], v[140:143]\n"
    "ds_read_b64_tr_b16 a[164:165], v3 offset:8192\n"
    "ds_read_b64_tr_b16 a[166:167], v3 offset:12288\n"
    "v_mfma_f32_16x16x32_bf16 v[140:143], a[152:155], a[24:27], v[140:143]\n"
    "v_exp_f32 v128, v128\n"
    "v_mfma_f32_16x16x32_bf16 v[140:143], a[156:159], a[28:31], v[140:143]\n"
    "ds_read_b64_tr_b16 a[168:169], v4 offset:0\n"
    "ds_read_b64_tr_b16 a[170:171], v4 offset:4096\n"
    "v_mfma_f32_16x16x32_bf16 v[156:159], a[144:147], a[32:35], v[216:219]\n"
    "v_exp_f32 v129, v129\n"
    "v_mfma_f32_16x16x32_bf16 v[156:159], a[148:151], a[36:39], v[156:159]\n"
    "ds_read_b64_tr_b16 a[172:173], v4 offset:8192\n"
    "ds_read_b64_tr_b16 a[174:175], v4 offset:12288\n"
    "v_mfma_f32_16x16x32_bf16 v[156:159], a[152:155], a[40:43], v[156:159]\n"
    "ds_read_b64_tr_b16 a[176:177], v3 offset:512\n"
    "ds_read_b64_tr_b16 a[178:179], v3 offset:4608\n"
    "v_mfma_f32_16x16x32_bf16 v[156:159], a[156:159], a[44:47], v[156:159]\n"
    "v_exp_f32 v130, v130\n"
    "v_mfma_f32_16x16x32_bf16 a[224:227], a[236:239], v[160:163], 0\n"
    "s_add_u32 s64, s59, 34816\n"
    "ds_read_b64_tr_b16 a[180:181], v3 offset:8704\n"
    "ds_read_b64_tr_b16 a[182:183], v3 offset:12800\n"
    "v_mfma_f32_16x16x32_bf16 a[224:227], a[236:239], v[164:167], a[224:227]\n"
    "s_add_u32 m0, s60, 34816\n"
    "v_exp_f32 v131, v131\n"
    "v_mfma_f32_16x16x32_bf16 a[228:231], a[236:239], v[176:179], 0\n"
    "buffer_load_dwordx4 v7, s[44:47], s64 offen nt lds\n"
    "ds_read_b128 a[152:155], v1 offset:47104\n"
    "v_mfma_f32_16x16x32_bf16 a[228:231], a[236:239], v[180:183], a[228:231]\n"
    "ds_read_b128 a[148:151], v2 offset:38912\n"
    "v_exp_f32 v144, v144\n"
    "v_mfma_f32_16x16x32_bf16 a[232:235], a[236:239], v[192:195], 0\n"
    "ds_read_b64_tr_b16 a[184:185], v4 offset:512\n"
    "v_cvt_pk_bf16_f32 v168, v112, v113\n"
    "v_mfma_f32_16x16x32_bf16 a[232:235], a[236:239], v[196:199], a[232:235]\n"
    "s_waitcnt lgkmcnt(5)\n"
    "ds_read_b128 a[144:147], v1 offset:38912\n"
    "v_exp_f32 v145, v145\n"
    "v_mfma_f32_16x16x32_bf16 v[16:19], a[160:163], v[160:163], 0\n"
    "ds_read_b64_tr_b16 a[186:187], v4 offset:4608\n"
    "v_cvt_pk_bf16_f32 v169, v114, v115\n"
    "v_mfma_f32_16x16x32_bf16 v[16:19], a[164:167], v[164:167], v[16:19]\n"
    "ds_read_b128 a[140:143], v2 offset:45056\n"
    "v_exp_f32 v146, v146\n"
    "v_mfma_f32_16x16x32_bf16 v[20:23], a[160:163], v[176:179], 0\n"
    "ds_read_b64_tr_b16 a[188:189], v4 offset:8704\n"
    "ds_read_b64_tr_b16 a[190:191], v4 offset:12800\n"
    "v_mfma_f32_16x16x32_bf16 v[20:23], a[164:167], v[180:183], v[20:23]\n"
    "v_exp_f32 v147, v147\n"
    "v_mfma_f32_16x16x32_bf16 v[24:27], a[160:163], v[192:195], 0\n"
    "ds_read_b64_tr_b16 a[192:193], v3 offset:1024\n"
    "v_cvt_pk_bf16_f32 v184, v128, v129\n"
    "v_mfma_f32_16x16x32_bf16 v[24:27], a[164:167], v[196:199], v[24:27]\n"
    "ds_read_b64_tr_b16 a[214:215], v3 offset:13824\n"
    "v_exp_f32 v116, v116\n"
    "v_mfma_f32_16x16x32_bf16 v[28:31], a[168:171], v[160:163], 0\n"
    "ds_read_b64_tr_b16 a[194:195], v3 offset:5120\n"
    "ds_read_b64_tr_b16 a[196:197], v3 offset:9216\n"
    "v_mfma_f32_16x16x32_bf16 v[28:31], a[172:175], v[164:167], v[28:31]\n"
    "v_exp_f32 v117, v117\n"
    "v_mfma_f32_16x16x32_bf16 v[32:35], a[168:171], v[176:179], 0\n"
    "ds_read_b128 a[96:99], v1 offset:32768\n"
    "v_cvt_pk_bf16_f32 v185, v130, v131\n"
    "v_mfma_f32_16x16x32_bf16 v[32:35], a[172:175], v[180:183], v[32:35]\n"
    "v_exp_f32 v118, v118\n"
    "v_mfma_f32_16x16x32_bf16 v[36:39], a[168:171], v[192:195], 0\n"
    "ds_read_b64_tr_b16 a[198:199], v3 offset:13312\n"
    "ds_read_b64_tr_b16 a[200:201], v4 offset:1024\n"
    "v_mfma_f32_16x16x32_bf16 v[36:39], a[172:175], v[196:199], v[36:39]\n"
    "ds_read_b64_tr_b16 a[220:221], v4 offset:9728\n"
    "ds_read_b64_tr_b16 a[222:223], v4 offset:13824\n"
    "v_exp_f32 v119, v119\n"
    "v_mfma_f32_16x16x32_bf16 v[40:43], a[176:179], v[160:163], 0\n"
    "s_waitcnt lgkmcnt(7)\n"
    "ds_read_b128 a[100:103], v2 offset:32768\n"
    "v_cvt_pk_bf16_f32 v200, v144, v145\n"
    "v_mfma_f32_16x16x32_bf16 v[40:43], a[180:183], v[164:167], v[40:43]\n"
    "v_exp_f32 v132, v132\n"
    "v_mfma_f32_16x16x32_bf16 v[44:47], a[176:179], v[176:179], 0\n"
    "ds_read_b128 a[104:107], v1 offset:40960\n"
    "v_cvt_pk_bf16_f32 v201, v146, v147\n"
    "v_mfma_f32_16x16x32_bf16 v[44:47], a[180:183], v[180:183], v[44:47]\n"
    "ds_read_b64_tr_b16 a[202:203], v4 offset:5120\n"
    "ds_read_b64_tr_b16 a[204:205], v4 offset:9216\n"
    "v_mfma_f32_16x16x32_bf16 v[48:51], a[176:179], v[192:195], 0\n"
    "v_exp_f32 v133, v133\n"
    "v_mfma_f32_16x16x32_bf16 v[48:51], a[180:183], v[196:199], v[48:51]\n"
    "s_add_u32 s65, s59, 34944\n"
    "ds_read_b128 a[108:111], v2 offset:40960\n"
    "v_cvt_pk_bf16_f32 v170, v116, v117\n"
    "v_mfma_f32_16x16x32_bf16 v[52:55], a[184:187], v[160:163], 0\n"
    "s_add_u32 m0, s60, 35840\n"
    "v_exp_f32 v134, v134\n"
    "v_mfma_f32_16x16x32_bf16 v[52:55], a[188:191], v[164:167], v[52:55]\n"
    "buffer_load_dwordx4 v7, s[44:47], s65 offen nt lds\n"
    "ds_read_b128 a[136:139], v1 offset:45056\n"
    "v_mfma_f32_16x16x32_bf16 v[56:59], a[184:187], v[176:179], 0\n"
    "v_exp_f32 v135, v135\n"
    "v_mfma_f32_16x16x32_bf16 v[56:59], a[188:191], v[180:183], v[56:59]\n"
    "ds_read_b64_tr_b16 a[206:207], v4 offset:13312\n"
    "ds_read_b64_tr_b16 a[208:209], v3 offset:1536\n"
    "v_mfma_f32_16x16x32_bf16 v[60:63], a[184:187], v[192:195], 0\n"
    "v_exp_f32 v148, v148\n"
    "v_mfma_f32_16x16x32_bf16 v[60:63], a[188:191], v[196:199], v[60:63]\n"
    "s_waitcnt lgkmcnt(4)\n"
    "ds_read_b128 a[112:115], v1 offset:34816\n"
    "v_cvt_pk_bf16_f32 v171, v118, v119\n"
    "v_mfma_f32_16x16x32_bf16 v[64:67], a[192:195], v[160:163], 0\n"
    "ds_read_b128 a[156:159], v2 offset:47104\n"
    "v_exp_f32 v149, v149\n"
    "v_mfma_f32_16x16x32_bf16 v[64:67], a[196:199], v[164:167], v[64:67]\n"
    "ds_read_b128 a[116:119], v2 offset:34816\n"
    "v_cvt_pk_bf16_f32 v186, v132, v133\n"
    "v_mfma_f32_16x16x32_bf16 v[68:71], a[192:195], v[176:179], 0\n"
    "v_exp_f32 v150, v150\n"
    "v_mfma_f32_16x16x32_bf16 v[68:71], a[196:199], v[180:183], v[68:71]\n"
    "ds_read_b64_tr_b16 a[210:211], v3 offset:5632\n"
    "ds_read_b64_tr_b16 a[212:213], v3 offset:9728\n"
    "v_mfma_f32_16x16x32_bf16 v[72:75], a[192:195], v[192:195], 0\n"
    "v_exp_f32 v151, v151\n"
    "v_mfma_f32_16x16x32_bf16 v[72:75], a[196:199], v[196:199], v[72:75]\n"
    "ds_read_b128 a[120:123], v1 offset:43008\n"
    "v_cvt_pk_bf16_f32 v187, v134, v135\n"
    "v_mfma_f32_16x16x32_bf16 v[76:79], a[200:203], v[160:163], 0\n"
    "s_waitcnt lgkmcnt(5)\n"
    "v_exp_f32 v120, v120\n"
    "v_mfma_f32_16x16x32_bf16 v[76:79], a[204:207], v[164:167], v[76:79]\n"
    "ds_read_b128 a[124:127], v2 offset:43008\n"
    "v_cvt_pk_bf16_f32 v202, v148, v149\n"
    "v_mfma_f32_16x16x32_bf16 v[80:83], a[200:203], v[176:179], 0\n"
    "v_exp_f32 v121, v121\n"
    "v_mfma_f32_16x16x32_bf16 v[80:83], a[204:207], v[180:183], v[80:83]\n"
    "ds_read_b128 a[128:131], v1 offset:36864\n"
    "v_cvt_pk_bf16_f32 v203, v150, v151\n"
    "v_mfma_f32_16x16x32_bf16 v[84:87], a[200:203], v[192:195], 0\n"
    "v_exp_f32 v122, v122\n"
    "v_mfma_f32_16x16x32_bf16 v[84:87], a[204:207], v[196:199], v[84:87]\n"
    "s_waitcnt lgkmcnt(3)\n"
    "ds_read_b64_tr_b16 a[216:217], v4 offset:1536\n"
    "ds_read_b64_tr_b16 a[218:219], v4 offset:5632\n"
    "v_mfma_f32_16x16x32_bf16 v[88:91], a[208:211], v[160:163], 0\n"
    "v_exp_f32 v123, v123\n"
    "v_mfma_f32_16x16x32_bf16 v[88:91], a[212:215], v[164:167], v[88:91]\n"
    "ds_read_b128 a[132:135], v2 offset:36864\n"
    "v_cvt_pk_bf16_f32 v172, v120, v121\n"
    "v_mfma_f32_16x16x32_bf16 v[92:95], a[208:211], v[176:179], 0\n"
    "v_exp_f32 v136, v136\n"
    "v_mfma_f32_16x16x32_bf16 v[92:95], a[212:215], v[180:183], v[92:95]\n"
    "v_cvt_pk_bf16_f32 v173, v122, v123\n"
    "v_mfma_f32_16x16x32_bf16 v[96:99], a[208:211], v[192:195], 0\n"
    "v_mfma_f32_16x16x32_bf16 v[96:99], a[212:215], v[196:199], v[96:99]\n"
    "s_waitcnt lgkmcnt(1)\n"
    "v_exp_f32 v137, v137\n"
    "v_mfma_f32_16x16x32_bf16 v[100:103], a[216:219], v[160:163], 0\n"
    "v_cvt_pk_bf16_f32 v188, v136, v137\n"
    "v_mfma_f32_16x16x32_bf16 v[100:103], a[220:223], v[164:167], v[100:103]\n"
    "v_exp_f32 v138, v138\n"
    "v_mfma_f32_16x16x32_bf16 v[104:107], a[216:219], v[176:179], 0\n"
    "v_mfma_f32_16x16x32_bf16 v[104:107], a[220:223], v[180:183], v[104:107]\n"
    "v_exp_f32 v139, v139\n"
    "v_mfma_f32_16x16x32_bf16 v[108:111], a[216:219], v[192:195], 0\n"
    "s_waitcnt vmcnt(4) lgkmcnt(0)\n"
    "v_cvt_pk_bf16_f32 v189, v138, v139\n"
    "v_mfma_f32_16x16x32_bf16 v[108:111], a[220:223], v[196:199], v[108:111]\n"
    "s_barrier\n"
    "v_exp_f32 v152, v152\n"
    "v_mfma_f32_16x16x32_bf16 v[112:115], a[96:99], a[0:3], v[208:211]\n"
    "s_add_u32 s62, s59, 49152\n"
    "v_mfma_f32_16x16x32_bf16 v[112:115], a[100:103], a[4:7], v[112:115]\n"
    "s_add_u32 m0, s60, 0\n"
    "v_exp_f32 v153, v153\n"
    "v_mfma_f32_16x16x32_bf16 v[112:115], a[104:107], a[8:11], v[112:115]\n"
    "buffer_load_dwordx4 v7, s[44:47], s62 offen nt lds\n"
    "v_mfma_f32_16x16x32_bf16 v[112:115], a[108:111], a[12:15], v[112:115]\n"
    "v_exp_f32 v154, v154\n"
    "v_mfma_f32_16x16x32_bf16 v[128:131], a[96:99], a[16:19], v[212:215]\n"
    "v_cvt_pk_bf16_f32 v204, v152, v153\n"
    "v_mfma_f32_16x16x32_bf16 v[128:131], a[100:103], a[20:23], v[128:131]\n"
    "v_exp_f32 v155, v155\n"
    "v_mfma_f32_16x16x32_bf16 v[128:131], a[104:107], a[24:27], v[128:131]\n"
    "s_add_u32 s63, s59, 49280\n"
    "v_cvt_pk_bf16_f32 v205, v154, v155\n"
    "v_mfma_f32_16x16x32_bf16 v[128:131], a[108:111], a[28:31], v[128:131]\n"
    "s_add_u32 m0, s60, 1024\n"
    "v_exp_f32 v124, v124\n"
    "v_mfma_f32_16x16x32_bf16 v[144:147], a[96:99], a[32:35], v[216:219]\n"
    "buffer_load_dwordx4 v7, s[44:47], s63 offen nt lds\n"
    "v_mfma_f32_16x16x32_bf16 v[144:147], a[100:103], a[36:39], v[144:147]\n"
    "v_exp_f32 v125, v125\n"
    "v_mfma_f32_16x16x32_bf16 v[144:147], a[104:107], a[40:43], v[144:147]\n"
    "ds_read_b64_tr_b16 a[222:223], v4 offset:30208\n"
    "v_cvt_pk_bf16_f32 v174, v124, v125\n"
    "v_mfma_f32_16x16x32_bf16 v[144:147], a[108:111], a[44:47], v[144:147]\n"
    "v_exp_f32 v126, v126\n"
    "v_mfma_f32_16x16x32_bf16 v[116:119], a[112:115], a[0:3], v[208:211]\n"
    "s_add_u32 s64, s59, 51200\n"
    "ds_read_b64_tr_b16 a[218:219], v4 offset:22016\n"
    "ds_read_b64_tr_b16 a[220:221], v4 offset:26112\n"
    "v_mfma_f32_16x16x32_bf16 v[116:119], a[116:119], a[4:7], v[116:119]\n"
    "s_add_u32 m0, s60, 2048\n"
    "v_exp_f32 v127, v127\n"
    "v_mfma_f32_16x16x32_bf16 v[116:119], a[120:123], a[8:11], v[116:119]\n"
    "buffer_load_dwordx4 v7, s[44:47], s64 offen nt lds\n"
    "v_mfma_f32_16x16x32_bf16 v[116:119], a[124:127], a[12:15], v[116:119]\n"
    "v_exp_f32 v140, v140\n"
    "v_mfma_f32_16x16x32_bf16 v[132:135], a[112:115], a[16:19], v[212:215]\n"
    "ds_read_b64_tr_b16 a[216:217], v4 offset:17920\n"
    "v_cvt_pk_bf16_f32 v175, v126, v127\n"
    "v_mfma_f32_16x16x32_bf16 v[132:135], a[116:119], a[20:23], v[132:135]\n"
    "ds_read_b64_tr_b16 a[212:213], v3 offset:26112\n"
    "ds_read_b64_tr_b16 a[214:215], v3 offset:30208\n"
    "v_mfma_f32_16x16x32_bf16 v[132:135], a[120:123], a[24:27], v[132:135]\n"
    "v_exp_f32 v141, v141\n"
    "v_mfma_f32_16x16x32_bf16 v[132:135], a[124:127], a[28:31], v[132:135]\n"
    "s_add_u32 s65, s59, 51328\n"
    "ds_read_b64_tr_b16 a[166:167], v3 offset:28672\n"
    "v_cvt_pk_bf16_f32 v190, v140, v141\n"
    "v_mfma_f32_16x16x32_bf16 v[148:151], a[112:115], a[32:35], v[216:219]\n"
    "s_add_u32 m0, s60, 3072\n"
    "v_exp_f32 v142, v142\n"
    "v_mfma_f32_16x16x32_bf16 v[148:151], a[116:119], a[36:39], v[148:151]\n"
    "buffer_load_dwordx4 v7, s[44:47], s65 offen nt lds\n"
    "v_mfma_f32_16x16x32_bf16 v[148:151], a[120:123], a[40:43], v[148:151]\n"
    "v_exp_f32 v143, v143\n"
    "v_mfma_f32_16x16x32_bf16 v[148:151], a[124:127], a[44:47], v[148:151]\n"
    "ds_read_b64_tr_b16 a[160:161], v3 offset:16384\n"
    "v_cvt_pk_bf16_f32 v191, v142, v143\n"
    "v_mfma_f32_16x16x32_bf16 v[120:123], a[128:131], a[0:3], v[208:211]\n"
    "v_exp_f32 v156, v156\n"
    "v_mfma_f32_16x16x32_bf16 v[120:123], a[132:135], a[4:7], v[120:123]\n"
    "s_add_u32 s62, s57, 81920\n"
    "ds_read_b64_tr_b16 a[162:163], v3 offset:20480\n"
    "ds_read_b64_tr_b16 a[164:165], v3 offset:24576\n"
    "v_mfma_f32_16x16x32_bf16 v[120:123], a[136:139], a[8:11], v[120:123]\n"
    "s_add_u32 m0, s58, 16384\n"
    "v_exp_f32 v157, v157\n"
    "v_mfma_f32_16x16x32_bf16 v[120:123], a[140:143], a[12:15], v[120:123]\n"
    "buffer_load_dwordx4 v5, s[40:43], s62 offen nt lds\n"
    "v_mfma_f32_16x16x32_bf16 v[136:139], a[128:131], a[16:19], v[212:215]\n"
    "v_exp_f32 v158, v158\n"
    "v_mfma_f32_16x16x32_bf16 v[136:139], a[132:135], a[20:23], v[136:139]\n"
    "ds_read_b64_tr_b16 a[168:169], v4 offset:16384\n"
    "v_cvt_pk_bf16_f32 v206, v156, v157\n"
    "v_mfma_f32_16x16x32_bf16 v[136:139], a[136:139], a[24:27], v[136:139]\n"
    "v_exp_f32 v159, v159\n"
    "v_mfma_f32_16x16x32_bf16 v[136:139], a[140:143], a[28:31], v[136:139]\n"
    "s_add_u32 s63, s57, 83968\n"
    "ds_read_b64_tr_b16 a[170:171], v4 offset:20480\n"
    "ds_read_b64_tr_b16 a[172:173], v4 offset:24576\n"
    "v_mfma_f32_16x16x32_bf16 v[152:155], a[128:131], a[32:35], v[216:219]\n"
    "s_add_u32 m0, s58, 17408\n"
    "v_exp_f32 v112, v112\n"
    "v_mfma_f32_16x16x32_bf16 v[152:155], a[132:135], a[36:39], v[152:155]\n"
    "buffer_load_dwordx4 v6, s[40:43], s63 offen nt lds\n"
    "v_mfma_f32_16x16x32_bf16 v[152:155], a[136:139], a[40:43], v[152:155]\n"
    "v_exp_f32 v113, v113\n"
    "v_mfma_f32_16x16x32_bf16 v[152:155], a[140:143], a[44:47], v[152:155]\n"
    "ds_read_b64_tr_b16 a[174:175], v4 offset:28672\n"
    "v_cvt_pk_bf16_f32 v207, v158, v159\n"
    "v_mfma_f32_16x16x32_bf16 v[124:127], a[144:147], a[0:3], v[208:211]\n"
    "v_exp_f32 v114, v114\n"
    "v_mfma_f32_16x16x32_bf16 v[124:127], a[148:151], a[4:7], v[124:127]\n"
    "s_add_u32 s64, s57, 86016\n"
    "ds_read_b64_tr_b16 a[176:177], v3 offset:16896\n"
    "ds_read_b64_tr_b16 a[178:179], v3 offset:20992\n"
    "v_mfma_f32_16x16x32_bf16 v[124:127], a[152:155], a[8:11], v[124:127]\n"
    "s_add_u32 m0, s58, 18432\n"
    "v_exp_f32 v115, v115\n"
    "v_mfma_f32_16x16x32_bf16 v[124:127], a[156:159], a[12:15], v[124:127]\n"
    "buffer_load_dwordx4 v5, s[40:43], s64 offen nt lds\n"
    "v_mfma_f32_16x16x32_bf16 v[140:143], a[144:147], a[16:19], v[212:215]\n"
    "v_exp_f32 v128, v128\n"
    "v_mfma_f32_16x16x32_bf16 v[140:143], a[148:151], a[20:23], v[140:143]\n"
    "ds_read_b64_tr_b16 a[180:181], v3 offset:25088\n"
    "v_cvt_pk_bf16_f32 v160, v112, v113\n"
    "v_mfma_f32_16x16x32_bf16 v[140:143], a[152:155], a[24:27], v[140:143]\n"
    "ds_read_b64_tr_b16 a[182:183], v3 offset:29184\n"
    "v_cvt_pk_bf16_f32 v161, v114, v115\n"
    "v_mfma_f32_16x16x32_bf16 v[140:143], a[156:159], a[28:31], v[140:143]\n"
    "v_exp_f32 v129, v129\n"
    "v_mfma_f32_16x16x32_bf16 v[156:159], a[144:147], a[32:35], v[216:219]\n"
    "s_add_u32 s65, s57, 88064\n"
    "ds_read_b64_tr_b16 a[184:185], v4 offset:16896\n"
    "ds_read_b64_tr_b16 a[186:187], v4 offset:20992\n"
    "v_mfma_f32_16x16x32_bf16 v[156:159], a[148:151], a[36:39], v[156:159]\n"
    "s_add_u32 m0, s58, 19456\n"
    "v_exp_f32 v130, v130\n"
    "v_mfma_f32_16x16x32_bf16 v[156:159], a[152:155], a[40:43], v[156:159]\n"
    "buffer_load_dwordx4 v6, s[40:43], s65 offen nt lds\n"
    "v_mfma_f32_16x16x32_bf16 v[156:159], a[156:159], a[44:47], v[156:159]\n"
    "v_exp_f32 v131, v131\n"
    "v_mfma_f32_16x16x32_bf16 a[224:227], a[236:239], v[168:171], a[224:227]\n"
    "ds_read_b64_tr_b16 a[188:189], v4 offset:25088\n"
    "ds_read_b64_tr_b16 a[190:191], v4 offset:29184\n"
    "v_mfma_f32_16x16x32_bf16 a[224:227], a[236:239], v[172:175], a[224:227]\n"
    "v_exp_f32 v144, v144\n"
    "v_mfma_f32_16x16x32_bf16 a[228:231], a[236:239], v[184:187], a[228:231]\n"
    "s_add_u32 s62, s57, 98304\n"
    "ds_read_b64_tr_b16 a[192:193], v3 offset:17408\n"
    "v_cvt_pk_bf16_f32 v176, v128, v129\n"
    "v_mfma_f32_16x16x32_bf16 a[228:231], a[236:239], v[188:191], a[228:231]\n"
    "s_add_u32 m0, s58, 32768\n"
    "v_exp_f32 v145, v145\n"
    "v_mfma_f32_16x16x32_bf16 a[232:235], a[236:239], v[200:203], a[232:235]\n"
    "buffer_load_dwordx4 v5, s[40:43], s62 offen nt lds\n"
    "v_mfma_f32_16x16x32_bf16 a[232:235], a[236:239], v[204:207], a[232:235]\n"
    "s_waitcnt lgkmcnt(3)\n"
    "v_exp_f32 v146, v146\n"
    "v_mfma_f32_16x16x32_bf16 v[16:19], a[160:163], v[168:171], v[16:19]\n"
    "ds_read_b64_tr_b16 a[194:195], v3 offset:21504\n"
    "v_cvt_pk_bf16_f32 v177, v130, v131\n"
    "v_mfma_f32_16x16x32_bf16 v[16:19], a[164:167], v[172:175], v[16:19]\n"
    "v_exp_f32 v147, v147\n"
    "v_mfma_f32_16x16x32_bf16 v[20:23], a[160:163], v[184:187], v[20:23]\n"
    "s_add_u32 s63, s57, 100352\n"
    "ds_read_b128 a[96:99], v1 offset:49152\n"
    "v_cvt_pk_bf16_f32 v192, v144, v145\n"
    "v_mfma_f32_16x16x32_bf16 v[20:23], a[164:167], v[188:191], v[20:23]\n"
    "s_add_u32 m0, s58, 33792\n"
    "v_exp_f32 v116, v116\n"
    "v_mfma_f32_16x16x32_bf16 v[24:27], a[160:163], v[200:203], v[24:27]\n"
    "buffer_load_dwordx4 v6, s[40:43], s63 offen nt lds\n"
    "v_mfma_f32_16x16x32_bf16 v[24:27], a[164:167], v[204:207], v[24:27]\n"
    "v_exp_f32 v117, v117\n"
    "v_mfma_f32_16x16x32_bf16 v[28:31], a[168:171], v[168:171], v[28:31]\n"
    "ds_read_b64_tr_b16 a[196:197], v3 offset:25600\n"
    "ds_read_b64_tr_b16 a[198:199], v3 offset:29696\n"
    "v_mfma_f32_16x16x32_bf16 v[28:31], a[172:175], v[172:175], v[28:31]\n"
    "v_exp_f32 v118, v118\n"
    "v_mfma_f32_16x16x32_bf16 v[32:35], a[168:171], v[184:187], v[32:35]\n"
    "s_add_u32 s64, s57, 102400\n"
    "ds_read_b128 a[100:103], v2 offset:49152\n"
    "v_cvt_pk_bf16_f32 v193, v146, v147\n"
    "v_mfma_f32_16x16x32_bf16 v[32:35], a[172:175], v[188:191], v[32:35]\n"
    "s_add_u32 m0, s58, 34816\n"
    "v_exp_f32 v119, v119\n"
    "v_mfma_f32_16x16x32_bf16 v[36:39], a[168:171], v[200:203], v[36:39]\n"
    "buffer_load_dwordx4 v5, s[40:43], s64 offen nt lds\n"
    "ds_read_b128 a[152:155], v1 offset:63488\n"
    "v_mfma_f32_16x16x32_bf16 v[36:39], a[172:175], v[204:207], v[36:39]\n"
    "ds_read_b128 a[136:139], v1 offset:61440\n"
    "v_exp_f32 v132, v132\n"
    "v_mfma_f32_16x16x32_bf16 v[40:43], a[176:179], v[168:171], v[40:43]\n"
    "ds_read_b128 a[104:107], v1 offset:57344\n"
    "v_cvt_pk_bf16_f32 v162, v116, v117\n"
    "v_mfma_f32_16x16x32_bf16 v[40:43], a[180:183], v[172:175], v[40:43]\n"
    "ds_read_b64_tr_b16 a[200:201], v4 offset:17408\n"
    "ds_read_b64_tr_b16 a[202:203], v4 offset:21504\n"
    "v_mfma_f32_16x16x32_bf16 v[44:47], a[176:179], v[184:187], v[44:47]\n"
    "Lpg1%=:\n"
    "s_cbranch_scc1 Lpg5%=\n"
    "v_exp_f32 v133, v133\n"
    "v_mfma_f32_16x16x32_bf16 v[44:47], a[180:183], v[188:191], v[44:47]\n"
    "s_add_u32 s65, s57, 104448\n"
    "ds_read_b128 a[108:111], v2 offset:57344\n"
    "v_cvt_pk_bf16_f32 v163, v118, v119\n"
    "v_mfma_f32_16x16x32_bf16 v[48:51], a[176:179], v[200:203], v[48:51]\n"
    "s_add_u32 m0, s58, 35840\n"
    "v_exp_f32 v134, v134\n"
    "v_mfma_f32_16x16x32_bf16 v[48:51], a[180:183], v[204:207], v[48:51]\n"
    "buffer_load_dwordx4 v6, s[40:43], s65 offen nt lds\n"
    "ds_read_b128 a[140:143], v2 offset:61440\n"
    "v_mfma_f32_16x16x32_bf16 v[52:55], a[184:187], v[168:171], v[52:55]\n"
    "s_waitcnt lgkmcnt(5)\n"
    "v_exp_f32 v135, v135\n"
    "v_mfma_f32_16x16x32_bf16 v[52:55], a[188:191], v[172:175], v[52:55]\n"
    "ds_read_b64_tr_b16 a[204:205], v4 offset:25600\n"
    "ds_read_b64_tr_b16 a[206:207], v4 offset:29696\n"
    "v_mfma_f32_16x16x32_bf16 v[56:59], a[184:187], v[184:187], v[56:59]\n"
    "v_exp_f32 v148, v148\n"
    "v_mfma_f32_16x16x32_bf16 v[56:59], a[188:191], v[188:191], v[56:59]\n"
    "s_add_u32 s62, s61, 49152\n"
    "ds_read_b128 a[112:115], v1 offset:51200\n"
    "v_cvt_pk_bf16_f32 v178, v132, v133\n"
    "v_mfma_f32_16x16x32_bf16 v[60:63], a[184:187], v[200:203], v[60:63]\n"
    "s_add_u32 m0, s78, 0\n"
    "v_exp_f32 v149, v149\n"
    "v_mfma_f32_16x16x32_bf16 v[60:63], a[188:191], v[204:207], v[60:63]\n"
    "buffer_load_dwordx4 v5, s[48:51], s62 offen nt lds\n"
    "ds_read_b128 a[144:147], v1 offset:55296\n"
    "v_mfma_f32_16x16x32_bf16 v[64:67], a[192:195], v[168:171], v[64:67]\n"
    "v_exp_f32 v150, v150\n"
    "v_mfma_f32_16x16x32_bf16 v[64:67], a[196:199], v[172:175], v[64:67]\n"
    "ds_read_b128 a[116:119], v2 offset:51200\n"
    "v_cvt_pk_bf16_f32 v179, v134, v135\n"
    "v_mfma_f32_16x16x32_bf16 v[68:71], a[192:195], v[184:187], v[68:71]\n"
    "v_exp_f32 v151, v151\n"
    "v_mfma_f32_16x16x32_bf16 v[68:71], a[196:199], v[188:191], v[68:71]\n"
    "s_add_u32 s63, s61, 51200\n"
    "ds_read_b64_tr_b16 a[208:209], v3 offset:17920\n"
    "ds_read_b64_tr_b16 a[210:211], v3 offset:22016\n"
    "v_mfma_f32_16x16x32_bf16 v[72:75], a[192:195], v[200:203], v[72:75]\n"
    "s_add_u32 m0, s78, 1024\n"
    "v_exp_f32 v120, v120\n"
    "v_mfma_f32_16x16x32_bf16 v[72:75], a[196:199], v[204:207], v[72:75]\n"
    "s_waitcnt lgkmcnt(4)\n"
    "buffer_load_dwordx4 v6, s[48:51], s63 offen nt lds\n"
    "ds_read_b128 a[148:151], v2 offset:55296\n"
    "v_mfma_f32_16x16x32_bf16 v[76:79], a[200:203], v[168:171], v[76:79]\n"
    "v_exp_f32 v121, v121\n"
    "v_mfma_f32_16x16x32_bf16 v[76:79], a[204:207], v[172:175], v[76:79]\n"
    "ds_read_b128 a[120:123], v1 offset:59392\n"
    "v_cvt_pk_bf16_f32 v194, v148, v149\n"
    "v_mfma_f32_16x16x32_bf16 v[80:83], a[200:203], v[184:187], v[80:83]\n"
    "v_exp_f32 v122, v122\n"
    "v_mfma_f32_16x16x32_bf16 v[80:83], a[204:207], v[188:191], v[80:83]\n"
    "s_add_u32 s64, s61, 49280\n"
    "ds_read_b128 a[124:127], v2 offset:59392\n"
    "v_cvt_pk_bf16_f32 v195, v150, v151\n"
    "v_mfma_f32_16x16x32_bf16 v[84:87], a[200:203], v[200:203], v[84:87]\n"
    "s_add_u32 m0, s78, 2048\n"
    "v_exp_f32 v123, v123\n"
    "v_mfma_f32_16x16x32_bf16 v[84:87], a[204:207], v[204:207], v[84:87]\n"
    "s_waitcnt lgkmcnt(3)\n"
    "ds_read_b128 a[128:131], v1 offset:53248\n"
    "v_cvt_pk_bf16_f32 v164, v120, v121\n"
    "v_mfma_f32_16x16x32_bf16 v[88:91], a[208:211], v[168:171], v[88:91]\n"
    "buffer_load_dwordx4 v5, s[48:51], s64 offen nt lds\n"
    "v_mfma_f32_16x16x32_bf16 v[88:91], a[212:215], v[172:175], v[88:91]\n"
    "ds_read_b128 a[156:159], v2 offset:63488\n"
    "v_exp_f32 v136, v136\n"
    "v_mfma_f32_16x16x32_bf16 v[92:95], a[208:211], v[184:187], v[92:95]\n"
    "ds_read_b128 a[132:135], v2 offset:53248\n"
    "v_cvt_pk_bf16_f32 v165, v122, v123\n"
    "v_mfma_f32_16x16x32_bf16 v[92:95], a[212:215], v[188:191], v[92:95]\n"
    "v_exp_f32 v137, v137\n"
    "v_mfma_f32_16x16x32_bf16 v[96:99], a[208:211], v[200:203], v[96:99]\n"
    "v_cvt_pk_bf16_f32 v180, v136, v137\n"
    "v_mfma_f32_16x16x32_bf16 v[96:99], a[212:215], v[204:207], v[96:99]\n"
    "v_exp_f32 v138, v138\n"
    "v_mfma_f32_16x16x32_bf16 v[100:103], a[216:219], v[168:171], v[100:103]\n"
    "v_mfma_f32_16x16x32_bf16 v[100:103], a[220:223], v[172:175], v[100:103]\n"
    "v_exp_f32 v139, v139\n"
    "v_mfma_f32_16x16x32_bf16 v[104:107], a[216:219], v[184:187], v[104:107]\n"
    "v_cvt_pk_bf16_f32 v181, v138, v139\n"
    "v_mfma_f32_16x16x32_bf16 v[104:107], a[220:223], v[188:191], v[104:107]\n"
    "v_exp_f32 v152, v152\n"
    "v_mfma_f32_16x16x32_bf16 v[108:111], a[216:219], v[200:203], v[108:111]\n"
    "s_waitcnt vmcnt(15) lgkmcnt(0)\n"
    "v_mfma_f32_16x16x32_bf16 v[108:111], a[220:223], v[204:207], v[108:111]\n"
    "s_barrier\n"
    "v_exp_f32 v153, v153\n"
    "v_mfma_f32_16x16x32_bf16 v[112:115], a[96:99], a[0:3], v[208:211]\n"
    "s_add_u32 s65, s59, 65536\n"
    "v_cvt_pk_bf16_f32 v196, v152, v153\n"
    "v_mfma_f32_16x16x32_bf16 v[112:115], a[100:103], a[4:7], v[112:115]\n"
    "s_add_u32 m0, s60, 16384\n"
    "v_exp_f32 v154, v154\n"
    "v_mfma_f32_16x16x32_bf16 v[112:115], a[104:107], a[8:11], v[112:115]\n"
    "buffer_load_dwordx4 v7, s[44:47], s65 offen nt lds\n"
    "v_mfma_f32_16x16x32_bf16 v[112:115], a[108:111], a[12:15], v[112:115]\n"
    "v_exp_f32 v155, v155\n"
    "v_mfma_f32_16x16x32_bf16 v[128:131], a[96:99], a[16:19], v[212:215]\n"
    "v_cvt_pk_bf16_f32 v197, v154, v155\n"
    "v_mfma_f32_16x16x32_bf16 v[128:131], a[100:103], a[20:23], v[128:131]\n"
    "v_exp_f32 v124, v124\n"
    "v_mfma_f32_16x16x32_bf16 v[128:131], a[104:107], a[24:27], v[128:131]\n"
    "v_mfma_f32_16x16x32_bf16 v[128:131], a[108:111], a[28:31], v[128:131]\n"
    "v_exp_f32 v125, v125\n"
    "v_mfma_f32_16x16x32_bf16 v[144:147], a[96:99], a[32:35], v[216:219]\n"
    "s_add_u32 s62, s59, 65664\n"
    "v_cvt_pk_bf16_f32 v166, v124, v125\n"
    "v_mfma_f32_16x16x32_bf16 v[144:147], a[100:103], a[36:39], v[144:147]\n"
    "s_add_u32 m0, s60, 17408\n"
    "v_exp_f32 v126, v126\n"
    "v_mfma_f32_16x16x32_bf16 v[144:147], a[104:107], a[40:43], v[144:147]\n"
    "buffer_load_dwordx4 v7, s[44:47], s62 offen nt lds\n"
    "v_mfma_f32_16x16x32_bf16 v[144:147], a[108:111], a[44:47], v[144:147]\n"
    "v_exp_f32 v127, v127\n"
    "v_mfma_f32_16x16x32_bf16 v[116:119], a[112:115], a[0:3], v[208:211]\n"
    "v_cvt_pk_bf16_f32 v167, v126, v127\n"
    "v_mfma_f32_16x16x32_bf16 v[116:119], a[116:119], a[4:7], v[116:119]\n"
    "v_mfma_f32_16x16x32_bf16 v[116:119], a[120:123], a[8:11], v[116:119]\n"
    "v_exp_f32 v140, v140\n"
    "v_mfma_f32_16x16x32_bf16 v[116:119], a[124:127], a[12:15], v[116:119]\n"
    "v_mfma_f32_16x16x32_bf16 v[132:135], a[112:115], a[16:19], v[212:215]\n"
    "v_exp_f32 v141, v141\n"
    "v_mfma_f32_16x16x32_bf16 v[132:135], a[116:119], a[20:23], v[132:135]\n"
    "s_add_u32 s63, s59, 67584\n"
    "ds_read_b64_tr_b16 a[222:223], v4 offset:46592\n"
    "v_cvt_pk_bf16_f32 v182, v140, v141\n"
    "v_mfma_f32_16x16x32_bf16 v[132:135], a[120:123], a[24:27], v[132:135]\n"
    "s_add_u32 m0, s60, 18432\n"
    "v_exp_f32 v142, v142\n"
    "v_mfma_f32_16x16x32_bf16 v[132:135], a[124:127], a[28:31], v[132:135]\n"
    "buffer_load_dwordx4 v7, s[44:47], s63 offen nt lds\n"
    "v_mfma_f32_16x16x32_bf16 v[148:151], a[112:115], a[32:35], v[216:219]\n"
    "v_exp_f32 v143, v143\n"
    "v_mfma_f32_16x16x32_bf16 v[148:151], a[116:119], a[36:39], v[148:151]\n"
    "ds_read_b64_tr_b16 a[220:221], v4 offset:42496\n"
    "v_cvt_pk_bf16_f32 v183, v142, v143\n"
    "v_mfma_f32_16x16x32_bf16 v[148:151], a[120:123], a[40:43], v[148:151]\n"
    "v_exp_f32 v156, v156\n"
    "v_mfma_f32_16x16x32_bf16 v[148:151], a[124:127], a[44:47], v[148:151]\n"
    "ds_read_b64_tr_b16 a[160:161], v3 offset:32768\n"
    "ds_read_b64_tr_b16 a[162:163], v3 offset:36864\n"
    "v_mfma_f32_16x16x32_bf16 v[120:123], a[128:131], a[0:3], v[208:211]\n"
    "v_exp_f32 v157, v157\n"
    "v_mfma_f32_16x16x32_bf16 v[120:123], a[132:135], a[4:7], v[120:123]\n"
    "s_add_u32 s64, s59, 67712\n"
    "ds_read_b64_tr_b16 a[164:165], v3 offset:40960\n"
    "ds_read_b64_tr_b16 a[166:167], v3 offset:45056\n"
    "v_mfma_f32_16x16x32_bf16 v[120:123], a[136:139], a[8:11], v[120:123]\n"
    "s_add_u32 m0, s60, 19456\n"
    "v_exp_f32 v158, v158\n"
    "v_mfma_f32_16x16x32_bf16 v[120:123], a[140:143], a[12:15], v[120:123]\n"
    "buffer_load_dwordx4 v7, s[44:47], s64 offen nt lds\n"
    "v_mfma_f32_16x16x32_bf16 v[136:139], a[128:131], a[16:19], v[212:215]\n"
    "v_exp_f32 v159, v159\n"
    "v_mfma_f32_16x16x32_bf16 v[136:139], a[132:135], a[20:23], v[136:139]\n"
    "ds_read_b64_tr_b16 a[168:169], v4 offset:32768\n"
    "v_cvt_pk_bf16_f32 v198, v156, v157\n"
    "v_mfma_f32_16x16x32_bf16 v[136:139], a[136:139], a[24:27], v[136:139]\n"
    "v_exp_f32 v112, v112\n"
    "v_mfma_f32_16x16x32_bf16 v[136:139], a[140:143], a[28:31], v[136:139]\n"
    "ds_read_b64_tr_b16 a[170:171], v4 offset:36864\n"
    "v_cvt_pk_bf16_f32 v199, v158, v159\n"
    "v_mfma_f32_16x16x32_bf16 v[152:155], a[128:131], a[32:35], v[216:219]\n"
    "v_exp_f32 v113, v113\n"
    "v_mfma_f32_16x16x32_bf16 v[152:155], a[132:135], a[36:39], v[152:155]\n"
    "ds_read_b64_tr_b16 a[172:173], v4 offset:40960\n"
    "ds_read_b64_tr_b16 a[174:175], v4 offset:45056\n"
    "v_mfma_f32_16x16x32_bf16 v[152:155], a[136:139], a[40:43], v[152:155]\n"
    "v_exp_f32 v114, v114\n"
    "v_mfma_f32_16x16x32_bf16 v[152:155], a[140:143], a[44:47], v[152:155]\n"
    "s_add_u32 s65, s57, 114688\n"
    "ds_read_b64_tr_b16 a[176:177], v3 offset:33280\n"
    "v_cvt_pk_bf16_f32 v168, v112, v113\n"
    "v_mfma_f32_16x16x32_bf16 v[124:127], a[144:147], a[0:3], v[208:211]\n"
    "s_add_u32 m0, s58, 49152\n"
    "v_exp_f32 v115, v115\n"
    "v_mfma_f32_16x16x32_bf16 v[124:127], a[148:151], a[4:7], v[124:127]\n"
    "buffer_load_dwordx4 v5, s[40:43], s65 offen nt lds\n"
    "v_mfma_f32_16x16x32_bf16 v[124:127], a[152:155], a[8:11], v[124:127]\n"
    "ds_read_b64_tr_b16 a[178:179], v3 offset:37376\n"
    "ds_read_b64_tr_b16 a[180:181], v3 offset:41472\n"
    "v_mfma_f32_16x16x32_bf16 v[124:127], a[156:159], a[12:15], v[124:127]\n"
    "v_exp_f32 v128, v128\n"
    "v_mfma_f32_16x16x32_bf16 v[140:143], a[144:147], a[16:19], v[212:215]\n"
    "ds_read_b64_tr_b16 a[182:183], v3 offset:45568\n"
    "v_cvt_pk_bf16_f32 v169, v114, v115\n"
    "v_mfma_f32_16x16x32_bf16 v[140:143], a[148:151], a[20:23], v[140:143]\n"
    "v_exp_f32 v129, v129\n"
    "v_mfma_f32_16x16x32_bf16 v[140:143], a[152:155], a[24:27], v[140:143]\n"
    "s_add_u32 s62, s57, 116736\n"
    "ds_read_b64_tr_b16 a[184:185], v4 offset:33280\n"
    "ds_read_b64_tr_b16 a[186:187], v4 offset:37376\n"
    "v_mfma_f32_16x16x32_bf16 v[140:143], a[156:159], a[28:31], v[140:143]\n"
    "s_add_u32 m0, s58, 50176\n"
    "v_exp_f32 v130, v130\n"
    "v_mfma_f32_16x16x32_bf16 v[156:159], a[144:147], a[32:35], v[216:219]\n"
    "buffer_load_dwordx4 v6, s[40:43], s62 offen nt lds\n"
    "v_mfma_f32_16x16x32_bf16 v[156:159], a[148:151], a[36:39], v[156:159]\n"
    "v_exp_f32 v131, v131\n"
    "v_mfma_f32_16x16x32_bf16 v[156:159], a[152:155], a[40:43], v[156:159]\n"
    "ds_read_b64_tr_b16 a[188:189], v4 offset:41472\n"
    "v_cvt_pk_bf16_f32 v184, v128, v129\n"
    "v_mfma_f32_16x16x32_bf16 v[156:159], a[156:159], a[44:47], v[156:159]\n"
    "v_exp_f32 v144, v144\n"
    "v_mfma_f32_16x16x32_bf16 a[224:227], a[236:239], v[160:163], a[224:227]\n"
    "ds_read_b64_tr_b16 a[190:191], v4 offset:45568\n"
    "v_cvt_pk_bf16_f32 v185, v130, v131\n"
    "v_mfma_f32_16x16x32_bf16 a[224:227], a[236:239], v[164:167], a[224:227]\n"
    "v_exp_f32 v145, v145\n"
    "v_mfma_f32_16x16x32_bf16 a[228:231], a[236:239], v[176:179], a[228:231]\n"
    "ds_read_b64_tr_b16 a[192:193], v3 offset:33792\n"
    "ds_read_b64_tr_b16 a[194:195], v3 offset:37888\n"
    "v_mfma_f32_16x16x32_bf16 a[228:231], a[236:239], v[180:183], a[228:231]\n"
    "s_add_u32 s63, s57, 118784\n"
    "v_exp_f32 v146, v146\n"
    "v_mfma_f32_16x16x32_bf16 a[232:235], a[236:239], v[192:195], a[232:235]\n"
    "s_add_u32 m0, s58, 51200\n"
    "ds_read_b128 a[96:99], v1 offset:0\n"
    "v_cvt_pk_bf16_f32 v200, v144, v145\n"
    "v_mfma_f32_16x16x32_bf16 a[232:235], a[236:239], v[196:199], a[232:235]\n"
    "s_waitcnt lgkmcnt(4)\n"
    "v_exp_f32 v147, v147\n"
    "v_mfma_f32_16x16x32_bf16 v[16:19], a[160:163], v[160:163], v[16:19]\n"
    "buffer_load_dwordx4 v5, s[40:43], s63 offen nt lds\n"
    "v_mfma_f32_16x16x32_bf16 v[16:19], a[164:167], v[164:167], v[16:19]\n"
    "ds_read_b128 a[156:159], v2 offset:14336\n"
    "v_exp_f32 v116, v116\n"
    "v_mfma_f32_16x16x32_bf16 v[20:23], a[160:163], v[176:179], v[20:23]\n"
    "ds_read_b64_tr_b16 a[196:197], v3 offset:41984\n"
    "ds_read_b64_tr_b16 a[198:199], v3 offset:46080\n"
    "v_mfma_f32_16x16x32_bf16 v[20:23], a[164:167], v[180:183], v[20:23]\n"
    "v_exp_f32 v117, v117\n"
    "v_mfma_f32_16x16x32_bf16 v[24:27], a[160:163], v[192:195], v[24:27]\n"
    "ds_read_b128 a[100:103], v2 offset:0\n"
    "v_cvt_pk_bf16_f32 v201, v146, v147\n"
    "v_mfma_f32_16x16x32_bf16 v[24:27], a[164:167], v[196:199], v[24:27]\n"
    "v_exp_f32 v118, v118\n"
    "v_mfma_f32_16x16x32_bf16 v[28:31], a[168:171], v[160:163], v[28:31]\n"
    "s_add_u32 s64, s57, 120832\n"
    "ds_read_b128 a[104:107], v1 offset:8192\n"
    "v_cvt_pk_bf16_f32 v170, v116, v117\n"
    "v_mfma_f32_16x16x32_bf16 v[28:31], a[172:175], v[164:167], v[28:31]\n"
    "s_add_u32 m0, s58, 52224\n"
    "v_exp_f32 v119, v119\n"
    "v_mfma_f32_16x16x32_bf16 v[32:35], a[168:171], v[176:179], v[32:35]\n"
    "buffer_load_dwordx4 v6, s[40:43], s64 offen nt lds\n"
    "ds_read_b128 a[148:151], v2 offset:6144\n"
    "v_mfma_f32_16x16x32_bf16 v[32:35], a[172:175], v[180:183], v[32:35]\n"
    "ds_read_b64_tr_b16 a[200:201], v4 offset:33792\n"
    "ds_read_b64_tr_b16 a[202:203], v4 offset:37888\n"
    "v_mfma_f32_16x16x32_bf16 v[36:39], a[168:171], v[192:195], v[36:39]\n"
    "v_exp_f32 v132, v132\n"
    "v_mfma_f32_16x16x32_bf16 v[36:39], a[172:175], v[196:199], v[36:39]\n"
    "ds_read_b128 a[108:111], v2 offset:8192\n"
    "v_cvt_pk_bf16_f32 v171, v118, v119\n"
    "v_mfma_f32_16x16x32_bf16 v[40:43], a[176:179], v[160:163], v[40:43]\n"
    "v_exp_f32 v133, v133\n"
    "v_mfma_f32_16x16x32_bf16 v[40:43], a[180:183], v[164:167], v[40:43]\n"
    "ds_read_b64_tr_b16 a[204:205], v4 offset:41984\n"
    "ds_read_b64_tr_b16 a[206:207], v4 offset:46080\n"
    "v_mfma_f32_16x16x32_bf16 v[44:47], a[176:179], v[176:179], v[44:47]\n"
    "v_exp_f32 v134, v134\n"
    "v_mfma_f32_16x16x32_bf16 v[44:47], a[180:183], v[180:183], v[44:47]\n"
    "s_add_u32 s65, s61, 51328\n"
    "ds_read_b128 a[112:115], v1 offset:2048\n"
    "v_cvt_pk_bf16_f32 v186, v132, v133\n"
    "v_mfma_f32_16x16x32_bf16 v[48:51], a[176:179], v[192:195], v[48:51]\n"
    "s_add_u32 m0, s78, 3072\n"
    "v_exp_f32 v135, v135\n"
    "v_mfma_f32_16x16x32_bf16 v[48:51], a[180:183], v[196:199], v[48:51]\n"
    "buffer_load_dwordx4 v6, s[48:51], s65 offen nt lds\n"
    "ds_read_b128 a[140:143], v2 offset:12288\n"
    "v_mfma_f32_16x16x32_bf16 v[52:55], a[184:187], v[160:163], v[52:55]\n"
    "s_waitcnt lgkmcnt(4)\n"
    "v_exp_f32 v148, v148\n"
    "v_mfma_f32_16x16x32_bf16 v[52:55], a[188:191], v[164:167], v[52:55]\n"
    "ds_read_b128 a[116:119], v2 offset:2048\n"
    "v_cvt_pk_bf16_f32 v187, v134, v135\n"
    "v_mfma_f32_16x16x32_bf16 v[56:59], a[184:187], v[176:179], v[56:59]\n"
    "v_exp_f32 v149, v149\n"
    "v_mfma_f32_16x16x32_bf16 v[56:59], a[188:191], v[180:183], v[56:59]\n"
    "ds_read_b64_tr_b16 a[208:209], v3 offset:34304\n"
    "ds_read_b64_tr_b16 a[210:211], v3 offset:38400\n"
    "v_mfma_f32_16x16x32_bf16 v[60:63], a[184:187], v[192:195], v[60:63]\n"
    "v_exp_f32 v150, v150\n"
    "v_mfma_f32_16x16x32_bf16 v[60:63], a[188:191], v[196:199], v[60:63]\n"
    "s_add_u32 s62, s61, 53248\n"
    "ds_read_b128 a[120:123], v1 offset:10240\n"
    "v_cvt_pk_bf16_f32 v202, v148, v149\n"
    "v_mfma_f32_16x16x32_bf16 v[64:67], a[192:195], v[160:163], v[64:67]\n"
    "s_add_u32 m0, s78, 4096\n"
    "v_exp_f32 v151, v151\n"
    "v_mfma_f32_16x16x32_bf16 v[64:67], a[196:199], v[164:167], v[64:67]\n"
    "buffer_load_dwordx4 v5, s[48:51], s62 offen nt lds\n"
    "ds_read_b128 a[144:147], v1 offset:6144\n"
    "v_mfma_f32_16x16x32_bf16 v[68:71], a[192:195], v[176:179], v[68:71]\n"
    "v_exp_f32 v120, v120\n"
    "v_mfma_f32_16x16x32_bf16 v[68:71], a[196:199], v[180:183], v[68:71]\n"
    "ds_read_b64_tr_b16 a[212:213], v3 offset:42496\n"
    "ds_read_b64_tr_b16 a[214:215], v3 offset:46592\n"
    "v_mfma_f32_16x16x32_bf16 v[72:75], a[192:195], v[192:195], v[72:75]\n"
    "v_exp_f32 v121, v121\n"
    "v_mfma_f32_16x16x32_bf16 v[72:75], a[196:199], v[196:199], v[72:75]\n"
    "ds_read_b128 a[124:127], v2 offset:10240\n"
    "v_cvt_pk_bf16_f32 v203, v150, v151\n"
    "v_mfma_f32_16x16x32_bf16 v[76:79], a[200:203], v[160:163], v[76:79]\n"
    "s_waitcnt lgkmcnt(4)\n"
    "v_exp_f32 v122, v122\n"
    "v_mfma_f32_16x16x32_bf16 v[76:79], a[204:207], v[164:167], v[76:79]\n"
    "ds_read_b128 a[128:131], v1 offset:4096\n"
    "v_cvt_pk_bf16_f32 v172, v120, v121\n"
    "v_mfma_f32_16x16x32_bf16 v[80:83], a[200:203], v[176:179], v[80:83]\n"
    "s_add_u32 s63, s61, 55296\n"
    "v_exp_f32 v123, v123\n"
    "v_mfma_f32_16x16x32_bf16 v[80:83], a[204:207], v[180:183], v[80:83]\n"
    "s_add_u32 m0, s78, 5120\n"
    "ds_read_b64_tr_b16 a[216:217], v4 offset:34304\n"
    "ds_read_b64_tr_b16 a[218:219], v4 offset:38400\n"
    "v_mfma_f32_16x16x32_bf16 v[84:87], a[200:203], v[192:195], v[84:87]\n"
    "buffer_load_dwordx4 v6, s[48:51], s63 offen nt lds\n"
    "v_mfma_f32_16x16x32_bf16 v[84:87], a[204:207], v[196:199], v[84:87]\n"
    "ds_read_b128 a[152:155], v1 offset:14336\n"
    "v_exp_f32 v136, v136\n"
    "v_mfma_f32_16x16x32_bf16 v[88:91], a[208:211], v[160:163], v[88:91]\n"
    "s_waitcnt lgkmcnt(4)\n"
    "ds_read_b128 a[132:135], v2 offset:4096\n"
    "v_cvt_pk_bf16_f32 v173, v122, v123\n"
    "v_mfma_f32_16x16x32_bf16 v[88:91], a[212:215], v[164:167], v[88:91]\n"
    "v_exp_f32 v137, v137\n"
    "v_mfma_f32_16x16x32_bf16 v[92:95], a[208:211], v[176:179], v[92:95]\n"
    "ds_read_b128 a[136:139], v1 offset:12288\n"
    "v_cvt_pk_bf16_f32 v188, v136, v137\n"
    "v_mfma_f32_16x16x32_bf16 v[92:95], a[212:215], v[180:183], v[92:95]\n"
    "v_exp_f32 v138, v138\n"
    "v_mfma_f32_16x16x32_bf16 v[96:99], a[208:211], v[192:195], v[96:99]\n"
    "v_mfma_f32_16x16x32_bf16 v[96:99], a[212:215], v[196:199], v[96:99]\n"
    "s_waitcnt lgkmcnt(3)\n"
    "v_exp_f32 v139, v139\n"
    "v_mfma_f32_16x16x32_bf16 v[100:103], a[216:219], v[160:163], v[100:103]\n"
    "v_cvt_pk_bf16_f32 v189, v138, v139\n"
    "v_mfma_f32_16x16x32_bf16 v[100:103], a[220:223], v[164:167], v[100:103]\n"
    "v_exp_f32 v152, v152\n"
    "v_mfma_f32_16x16x32_bf16 v[104:107], a[216:219], v[176:179], v[104:107]\n"
    "v_mfma_f32_16x16x32_bf16 v[104:107], a[220:223], v[180:183], v[104:107]\n"
    "v_exp_f32 v153, v153\n"
    "v_mfma_f32_16x16x32_bf16 v[108:111], a[216:219], v[192:195], v[108:111]\n"
    "s_waitcnt vmcnt(18) lgkmcnt(0)\n"
    "v_cvt_pk_bf16_f32 v204, v152, v153\n"
    "v_mfma_f32_16x16x32_bf16 v[108:111], a[220:223], v[196:199], v[108:111]\n"
    "s_barrier\n"
    "v_exp_f32 v154, v154\n"
    "v_mfma_f32_16x16x32_bf16 v[112:115], a[96:99], a[0:3], v[208:211]\n"
    "s_add_u32 s64, s59, 81920\n"
    "v_mfma_f32_16x16x32_bf16 v[112:115], a[100:103], a[4:7], v[112:115]\n"
    "s_add_u32 m0, s60, 32768\n"
    "v_exp_f32 v155, v155\n"
    "v_mfma_f32_16x16x32_bf16 v[112:115], a[104:107], a[8:11], v[112:115]\n"
    "buffer_load_dwordx4 v7, s[44:47], s64 offen nt lds\n"
    "v_mfma_f32_16x16x32_bf16 v[112:115], a[108:111], a[12:15], v[112:115]\n"
    "v_exp_f32 v124, v124\n"
    "v_mfma_f32_16x16x32_bf16 v[128:131], a[96:99], a[16:19], v[212:215]\n"
    "v_cvt_pk_bf16_f32 v205, v154, v155\n"
    "v_mfma_f32_16x16x32_bf16 v[128:131], a[100:103], a[20:23], v[128:131]\n"
    "v_exp_f32 v125, v125\n"
    "v_mfma_f32_16x16x32_bf16 v[128:131], a[104:107], a[24:27], v[128:131]\n"
    "v_cvt_pk_bf16_f32 v174, v124, v125\n"
    "v_mfma_f32_16x16x32_bf16 v[128:131], a[108:111], a[28:31], v[128:131]\n"
    "v_exp_f32 v126, v126\n"
    "v_mfma_f32_16x16x32_bf16 v[144:147], a[96:99], a[32:35], v[216:219]\n"
    "s_add_u32 s65, s59, 82048\n"
    "v_mfma_f32_16x16x32_bf16 v[144:147], a[100:103], a[36:39], v[144:147]\n"
    "s_waitcnt vmcnt(13)\n"
    "ds_read_b128 v[220:223], v8 offset:0\n"
    "v_mfma_f32_16x16x32_bf16 v[144:147], a[104:107], a[40:43], v[144:147]\n"
    "s_add_u32 m0, s60, 33792\n"
    "v_exp_f32 v127, v127\n"
    "v_mfma_f32_16x16x32_bf16 v[144:147], a[108:111], a[44:47], v[144:147]\n"
    "buffer_load_dwordx4 v7, s[44:47], s65 offen nt lds\n"
    "v_mfma_f32_16x16x32_bf16 v[116:119], a[112:115], a[0:3], v[208:211]\n"
    "v_exp_f32 v140, v140\n"
    "v_mfma_f32_16x16x32_bf16 v[116:119], a[116:119], a[4:7], v[116:119]\n"
    "v_cvt_pk_bf16_f32 v175, v126, v127\n"
    "v_mfma_f32_16x16x32_bf16 v[116:119], a[120:123], a[8:11], v[116:119]\n"
    "v_exp_f32 v141, v141\n"
    "v_mfma_f32_16x16x32_bf16 v[116:119], a[124:127], a[12:15], v[116:119]\n"
    "s_waitcnt lgkmcnt(0)\n"
    "v_cvt_pk_bf16_f32 v190, v140, v141\n"
    "v_lshlrev_b32 v246, 16, v220\n"
    "v_mfma_f32_16x16x32_bf16 v[132:135], a[112:115], a[16:19], v[212:215]\n"
    "v_exp_f32 v142, v142\n"
    "v_and_b32 v220, 0xffff0000, v220\n"
    "v_mfma_f32_16x16x32_bf16 v[132:135], a[116:119], a[20:23], v[132:135]\n"
    "s_add_u32 s62, s59, 83968\n"
    "ds_read_b64_tr_b16 a[220:221], v4 offset:9728\n"
    "ds_read_b64_tr_b16 a[222:223], v4 offset:13824\n"
    "v_mul_f32 v246, s56, v246\n"
    "v_mfma_f32_16x16x32_bf16 v[132:135], a[120:123], a[24:27], v[132:135]\n"
    "s_add_u32 m0, s60, 34816\n"
    "v_exp_f32 v143, v143\n"
    "v_mul_f32 v220, s56, v220\n"
    "v_mfma_f32_16x16x32_bf16 v[132:135], a[124:127], a[28:31], v[132:135]\n"
    "buffer_load_dwordx4 v7, s[44:47], s62 offen nt lds\n"
    "v_cvt_pk_bf16_f32 v220, v246, v220\n"
    "v_mfma_f32_16x16x32_bf16 v[148:151], a[112:115], a[32:35], v[216:219]\n"
    "v_exp_f32 v156, v156\n"
    "v_accvgpr_write_b32 a48, v220\n"
    "v_lshlrev_b32 v246, 16, v221\n"
    "v_mfma_f32_16x16x32_bf16 v[148:151], a[116:119], a[36:39], v[148:151]\n"
    "ds_read_b64_tr_b16 a[166:167], v3 offset:12288\n"
    "v_cvt_pk_bf16_f32 v191, v142, v143\n"
    "v_and_b32 v221, 0xffff0000, v221\n"
    "v_mfma_f32_16x16x32_bf16 v[148:151], a[120:123], a[40:43], v[148:151]\n"
    "v_exp_f32 v157, v157\n"
    "v_mul_f32 v246, s56, v246\n"
    "v_mfma_f32_16x16x32_bf16 v[148:151], a[124:127], a[44:47], v[148:151]\n"
    "ds_read_b64_tr_b16 a[160:161], v3 offset:0\n"
    "ds_read_b64_tr_b16 a[162:163], v3 offset:4096\n"
    "v_mul_f32 v221, s56, v221\n"
    "v_mfma_f32_16x16x32_bf16 v[120:123], a[128:131], a[0:3], v[208:211]\n"
    "v_exp_f32 v158, v158\n"
    "v_cvt_pk_bf16_f32 v221, v246, v221\n"
    "v_mfma_f32_16x16x32_bf16 v[120:123], a[132:135], a[4:7], v[120:123]\n"
    "s_add_u32 s63, s59, 84096\n"
    "ds_read_b64_tr_b16 a[164:165], v3 offset:8192\n"
    "v_cvt_pk_bf16_f32 v206, v156, v157\n"
    "v_accvgpr_write_b32 a49, v221\n"
    "v_mfma_f32_16x16x32_bf16 v[120:123], a[136:139], a[8:11], v[120:123]\n"
    "s_add_u32 m0, s60, 35840\n"
    "v_exp_f32 v159, v159\n"
    "v_lshlrev_b32 v246, 16, v222\n"
    "v_mfma_f32_16x16x32_bf16 v[120:123], a[140:143], a[12:15], v[120:123]\n"
    "buffer_load_dwordx4 v7, s[44:47], s63 offen nt lds\n"
    "v_and_b32 v222, 0xffff0000, v222\n"
    "v_mfma_f32_16x16x32_bf16 v[136:139], a[128:131], a[16:19], v[212:215]\n"
    "v_exp_f32 v112, v112\n"
    "v_mul_f32 v246, s56, v246\n"
    "v_mfma_f32_16x16x32_bf16 v[136:139], a[132:135], a[20:23], v[136:139]\n"
    "ds_read_b64_tr_b16 a[168:169], v4 offset:0\n"
    "v_cvt_pk_bf16_f32 v207, v158, v159\n"
    "v_mul_f32 v222, s56, v222\n"
    "v_mfma_f32_16x16x32_bf16 v[136:139], a[136:139], a[24:27], v[136:139]\n"
    "v_exp_f32 v113, v113\n"
    "v_cvt_pk_bf16_f32 v222, v246, v222\n"
    "v_mfma_f32_16x16x32_bf16 v[136:139], a[140:143], a[28:31], v[136:139]\n"
    "ds_read_b64_tr_b16 a[170:171], v4 offset:4096\n"
    "ds_read_b64_tr_b16 a[172:173], v4 offset:8192\n"
    "v_accvgpr_write_b32 a50, v222\n"
    "v_mfma_f32_16x16x32_bf16 v[152:155], a[128:131], a[32:35], v[216:219]\n"
    "v_exp_f32 v114, v114\n"
    "v_lshlrev_b32 v246, 16, v223\n"
    "v_and_b32 v223, 0xffff0000, v223\n"
    "v_mfma_f32_16x16x32_bf16 v[152:155], a[132:135], a[36:39], v[152:155]\n"
    "ds_read_b64_tr_b16 a[174:175], v4 offset:12288\n"
    "v_cvt_pk_bf16_f32 v160, v112, v113\n"
    "v_mul_f32 v246, s56, v246\n"
    "v_mfma_f32_16x16x32_bf16 v[152:155], a[136:139], a[40:43], v[152:155]\n"
    "s_add_u32 s64, s57, 0\n"
    "ds_read_b64_tr_b16 a[176:177], v3 offset:512\n"
    "ds_read_b64_tr_b16 a[178:179], v3 offset:4608\n"
    "v_mul_f32 v223, s56, v223\n"
    "v_mfma_f32_16x16x32_bf16 v[152:155], a[140:143], a[44:47], v[152:155]\n"
    "s_add_u32 m0, s58, 0\n"
    "v_exp_f32 v115, v115\n"
    "v_cvt_pk_bf16_f32 v223, v246, v223\n"
    "v_mfma_f32_16x16x32_bf16 v[124:127], a[144:147], a[0:3], v[208:211]\n"
    "buffer_load_dwordx4 v5, s[40:43], s64 offen nt lds\n"
    "v_accvgpr_write_b32 a51, v223\n"
    "v_mfma_f32_16x16x32_bf16 v[124:127], a[148:151], a[4:7], v[124:127]\n"
    "ds_read_b128 v[224:227], v9 offset:0\n"
    "v_exp_f32 v128, v128\n"
    "v_mfma_f32_16x16x32_bf16 v[124:127], a[152:155], a[8:11], v[124:127]\n"
    "ds_read_b64_tr_b16 a[180:181], v3 offset:8704\n"
    "v_cvt_pk_bf16_f32 v161, v114, v115\n"
    "v_mfma_f32_16x16x32_bf16 v[124:127], a[156:159], a[12:15], v[124:127]\n"
    "v_exp_f32 v129, v129\n"
    "v_mfma_f32_16x16x32_bf16 v[140:143], a[144:147], a[16:19], v[212:215]\n"
    "ds_read_b64_tr_b16 a[182:183], v3 offset:12800\n"
    "v_cvt_pk_bf16_f32 v176, v128, v129\n"
    "v_mfma_f32_16x16x32_bf16 v[140:143], a[148:151], a[20:23], v[140:143]\n"
    "v_exp_f32 v130, v130\n"
    "v_mfma_f32_16x16x32_bf16 v[140:143], a[152:155], a[24:27], v[140:143]\n"
    "s_add_u32 s65, s57, 2048\n"
    "ds_read_b64_tr_b16 a[184:185], v4 offset:512\n"
    "ds_read_b64_tr_b16 a[186:187], v4 offset:4608\n"
    "v_mfma_f32_16x16x32_bf16 v[140:143], a[156:159], a[28:31], v[140:143]\n"
    "s_add_u32 m0, s58, 1024\n"
    "v_exp_f32 v131, v131\n"
    "v_mfma_f32_16x16x32_bf16 v[156:159], a[144:147], a[32:35], v[216:219]\n"
    "s_waitcnt lgkmcnt(4)\n"
    "buffer_load_dwordx4 v6, s[40:43], s65 offen nt lds\n"
    "v_lshlrev_b32 v246, 16, v224\n"
    "v_mfma_f32_16x16x32_bf16 v[156:159], a[148:151], a[36:39], v[156:159]\n"
    "v_exp_f32 v144, v144\n"
    "v_and_b32 v224, 0xffff0000, v224\n"
    "v_mfma_f32_16x16x32_bf16 v[156:159], a[152:155], a[40:43], v[156:159]\n"
    "ds_read_b64_tr_b16 a[188:189], v4 offset:8704\n"
    "ds_read_b64_tr_b16 a[190:191], v4 offset:12800\n"
    "v_mul_f32 v246, s56, v246\n"
    "v_mfma_f32_16x16x32_bf16 v[156:159], a[156:159], a[44:47], v[156:159]\n"
    "v_exp_f32 v145, v145\n"
    "v_mul_f32 v224, s56, v224\n"
    "v_mfma_f32_16x16x32_bf16 a[224:227], a[236:239], v[168:171], a[224:227]\n"
    "ds_read_b64_tr_b16 a[192:193], v3 offset:1024\n"
    "v_cvt_pk_bf16_f32 v177, v130, v131\n"
    "v_cvt_pk_bf16_f32 v224, v246, v224\n"
    "v_mfma_f32_16x16x32_bf16 a[224:227], a[236:239], v[172:175], a[224:227]\n"
    "v_exp_f32 v146, v146\n"
    "v_accvgpr_write_b32 a52, v224\n"
    "v_mfma_f32_16x16x32_bf16 a[228:231], a[236:239], v[184:187], a[228:231]\n"
    "ds_read_b64_tr_b16 a[194:195], v3 offset:5120\n"
    "ds_read_b64_tr_b16 a[196:197], v3 offset:9216\n"
    "v_lshlrev_b32 v246, 16, v225\n"
    "v_and_b32 v225, 0xffff0000, v225\n"
    "v_mfma_f32_16x16x32_bf16 a[228:231], a[236:239], v[188:191], a[228:231]\n"
    "v_exp_f32 v147, v147\n"
    "v_mul_f32 v246, s56, v246\n"
    "v_mfma_f32_16x16x32_bf16 a[232:235], a[236:239], v[200:203], a[232:235]\n"
    "s_add_u32 s62, s57, 4096\n"
    "ds_read_b128 a[96:99], v1 offset:16384\n"
    "v_cvt_pk_bf16_f32 v192, v144, v145\n"
    "v_mul_f32 v225, s56, v225\n"
    "v_mfma_f32_16x16x32_bf16 a[232:235], a[236:239], v[204:207], a[232:235]\n"
    "s_add_u32 m0, s58, 2048\n"
    "v_exp_f32 v116, v116\n"
    "v_cvt_pk_bf16_f32 v225, v246, v225\n"
    "v_mfma_f32_16x16x32_bf16 v[16:19], a[160:163], v[168:171], v[16:19]\n"
    "buffer_load_dwordx4 v5, s[40:43], s62 offen nt lds\n"
    "v_accvgpr_write_b32 a53, v225\n"
    "v_mfma_f32_16x16x32_bf16 v[16:19], a[164:167], v[172:175], v[16:19]\n"
    "v_exp_f32 v117, v117\n"
    "v_lshlrev_b32 v246, 16, v226\n"
    "v_mfma_f32_16x16x32_bf16 v[20:23], a[160:163], v[184:187], v[20:23]\n"
    "ds_read_b64_tr_b16 a[198:199], v3 offset:13312\n"
    "v_cvt_pk_bf16_f32 v193, v146, v147\n"
    "v_and_b32 v226, 0xffff0000, v226\n"
    "v_mfma_f32_16x16x32_bf16 v[20:23], a[164:167], v[188:191], v[20:23]\n"
    "v_exp_f32 v118, v118\n"
    "v_mul_f32 v246, s56, v246\n"
    "v_mfma_f32_16x16x32_bf16 v[24:27], a[160:163], v[200:203], v[24:27]\n"
    "ds_read_b128 a[100:103], v2 offset:16384\n"
    "v_cvt_pk_bf16_f32 v162, v116, v117\n"
    "v_mul_f32 v226, s56, v226\n"
    "v_mfma_f32_16x16x32_bf16 v[24:27], a[164:167], v[204:207], v[24:27]\n"
    "s_add_u32 s63, s57, 6144\n"
    "ds_read_b64_tr_b16 a[200:201], v4 offset:1024\n"
    "ds_read_b64_tr_b16 a[202:203], v4 offset:5120\n"
    "v_cvt_pk_bf16_f32 v226, v246, v226\n"
    "v_mfma_f32_16x16x32_bf16 v[28:31], a[168:171], v[168:171], v[28:31]\n"
    "s_add_u32 m0, s58, 3072\n"
    "v_exp_f32 v119, v119\n"
    "v_accvgpr_write_b32 a54, v226\n"
    "v_lshlrev_b32 v246, 16, v227\n"
    "v_mfma_f32_16x16x32_bf16 v[28:31], a[172:175], v[172:175], v[28:31]\n"
    "buffer_load_dwordx4 v6, s[40:43], s63 offen nt lds\n"
    "ds_read_b128 a[156:159], v2 offset:30720\n"
    "v_and_b32 v227, 0xffff0000, v227\n"
    "v_mfma_f32_16x16x32_bf16 v[32:35], a[168:171], v[184:187], v[32:35]\n"
    "ds_read_b128 a[148:151], v2 offset:22528\n"
    "v_exp_f32 v132, v132\n"
    "v_mul_f32 v246, s56, v246\n"
    "v_mfma_f32_16x16x32_bf16 v[32:35], a[172:175], v[188:191], v[32:35]\n"
    "ds_read_b128 a[104:107], v1 offset:24576\n"
    "v_cvt_pk_bf16_f32 v163, v118, v119\n"
    "v_mul_f32 v227, s56, v227\n"
    "v_mfma_f32_16x16x32_bf16 v[36:39], a[168:171], v[200:203], v[36:39]\n"
    "v_exp_f32 v133, v133\n"
    "v_cvt_pk_bf16_f32 v227, v246, v227\n"
    "v_mfma_f32_16x16x32_bf16 v[36:39], a[172:175], v[204:207], v[36:39]\n"
    "ds_read_b128 a[108:111], v2 offset:24576\n"
    "v_cvt_pk_bf16_f32 v178, v132, v133\n"
    "v_accvgpr_write_b32 a55, v227\n"
    "v_mfma_f32_16x16x32_bf16 v[40:43], a[176:179], v[168:171], v[40:43]\n"
    "s_waitcnt lgkmcnt(4)\n"
    "v_exp_f32 v134, v134\n"
    "v_mfma_f32_16x16x32_bf16 v[40:43], a[180:183], v[172:175], v[40:43]\n"
    "ds_read_b64_tr_b16 a[204:205], v4 offset:9216\n"
    "ds_read_b64_tr_b16 a[206:207], v4 offset:13312\n"
    "v_mfma_f32_16x16x32_bf16 v[44:47], a[176:179], v[184:187], v[44:47]\n"
    "Lpg2%=:\n"
    "s_cbranch_scc1 Lpg6%=\n"
    "v_exp_f32 v135, v135\n"
    "v_mfma_f32_16x16x32_bf16 v[44:47], a[180:183], v[188:191], v[44:47]\n"
    "s_add_u32 s64, s61, 53376\n"
    "ds_read_b128 a[112:115], v1 offset:18432\n"
    "v_cvt_pk_bf16_f32 v179, v134, v135\n"
    "v_mfma_f32_16x16x32_bf16 v[48:51], a[176:179], v[200:203], v[48:51]\n"
    "s_add_u32 m0, s78, 6144\n"
    "v_exp_f32 v148, v148\n"
    "v_mfma_f32_16x16x32_bf16 v[48:51], a[180:183], v[204:207], v[48:51]\n"
    "buffer_load_dwordx4 v5, s[48:51], s64 offen nt lds\n"
    "ds_read_b128 a[140:143], v2 offset:28672\n"
    "v_mfma_f32_16x16x32_bf16 v[52:55], a[184:187], v[168:171], v[52:55]\n"
    "v_exp_f32 v149, v149\n"
    "v_mfma_f32_16x16x32_bf16 v[52:55], a[188:191], v[172:175], v[52:55]\n"
    "ds_read_b128 a[116:119], v2 offset:18432\n"
    "v_cvt_pk_bf16_f32 v194, v148, v149\n"
    "v_mfma_f32_16x16x32_bf16 v[56:59], a[184:187], v[184:187], v[56:59]\n"
    "v_exp_f32 v150, v150\n"
    "v_mfma_f32_16x16x32_bf16 v[56:59], a[188:191], v[188:191], v[56:59]\n"
    "ds_read_b64_tr_b16 a[208:209], v3 offset:1536\n"
    "ds_read_b64_tr_b16 a[210:211], v3 offset:5632\n"
    "v_mfma_f32_16x16x32_bf16 v[60:63], a[184:187], v[200:203], v[60:63]\n"
    "v_exp_f32 v151, v151\n"
    "v_mfma_f32_16x16x32_bf16 v[60:63], a[188:191], v[204:207], v[60:63]\n"
    "s_add_u32 s65, s61, 55424\n"
    "ds_read_b128 a[120:123], v1 offset:26624\n"
    "v_cvt_pk_bf16_f32 v195, v150, v151\n"
    "v_mfma_f32_16x16x32_bf16 v[64:67], a[192:195], v[168:171], v[64:67]\n"
    "s_add_u32 m0, s78, 7168\n"
    "v_exp_f32 v120, v120\n"
    "v_mfma_f32_16x16x32_bf16 v[64:67], a[196:199], v[172:175], v[64:67]\n"
    "buffer_load_dwordx4 v6, s[48:51], s65 offen nt lds\n"
    "ds_read_b128 a[144:147], v1 offset:22528\n"
    "v_mfma_f32_16x16x32_bf16 v[68:71], a[192:195], v[184:187], v[68:71]\n"
    "v_exp_f32 v121, v121\n"
    "v_mfma_f32_16x16x32_bf16 v[68:71], a[196:199], v[188:191], v[68:71]\n"
    "ds_read_b64_tr_b16 a[212:213], v3 offset:9728\n"
    "ds_read_b64_tr_b16 a[214:215], v3 offset:13824\n"
    "v_mfma_f32_16x16x32_bf16 v[72:75], a[192:195], v[200:203], v[72:75]\n"
    "v_exp_f32 v122, v122\n"
    "v_mfma_f32_16x16x32_bf16 v[72:75], a[196:199], v[204:207], v[72:75]\n"
    "ds_read_b128 a[124:127], v2 offset:26624\n"
    "v_cvt_pk_bf16_f32 v164, v120, v121\n"
    "v_mfma_f32_16x16x32_bf16 v[76:79], a[200:203], v[168:171], v[76:79]\n"
    "s_waitcnt lgkmcnt(4)\n"
    "ds_read_b64_tr_b16 a[216:217], v4 offset:1536\n"
    "ds_read_b64_tr_b16 a[218:219], v4 offset:5632\n"
    "v_mfma_f32_16x16x32_bf16 v[76:79], a[204:207], v[172:175], v[76:79]\n"
    "v_exp_f32 v123, v123\n"
    "v_mfma_f32_16x16x32_bf16 v[80:83], a[200:203], v[184:187], v[80:83]\n"
    "s_add_u32 s62, s61, 57344\n"
    "ds_read_b128 a[128:131], v1 offset:20480\n"
    "v_cvt_pk_bf16_f32 v165, v122, v123\n"
    "v_mfma_f32_16x16x32_bf16 v[80:83], a[204:207], v[188:191], v[80:83]\n"
    "s_add_u32 m0, s78, 8192\n"
    "v_exp_f32 v136, v136\n"
    "v_mfma_f32_16x16x32_bf16 v[84:87], a[200:203], v[200:203], v[84:87]\n"
    "buffer_load_dwordx4 v5, s[48:51], s62 offen nt lds\n"
    "ds_read_b128 a[152:155], v1 offset:30720\n"
    "v_mfma_f32_16x16x32_bf16 v[84:87], a[204:207], v[204:207], v[84:87]\n"
    "v_exp_f32 v137, v137\n"
    "v_mfma_f32_16x16x32_bf16 v[88:91], a[208:211], v[168:171], v[88:91]\n"
    "s_waitcnt lgkmcnt(4)\n"
    "ds_read_b128 a[132:135], v2 offset:20480\n"
    "v_cvt_pk_bf16_f32 v180, v136, v137\n"
    "v_mfma_f32_16x16x32_bf16 v[88:91], a[212:215], v[172:175], v[88:91]\n"
    "v_exp_f32 v138, v138\n"
    "v_mfma_f32_16x16x32_bf16 v[92:95], a[208:211], v[184:187], v[92:95]\n"
    "ds_read_b128 a[136:139], v1 offset:28672\n"
    "v_mfma_f32_16x16x32_bf16 v[92:95], a[212:215], v[188:191], v[92:95]\n"
    "v_exp_f32 v139, v139\n"
    "v_mfma_f32_16x16x32_bf16 v[96:99], a[208:211], v[200:203], v[96:99]\n"
    "v_cvt_pk_bf16_f32 v181, v138, v139\n"
    "v_mfma_f32_16x16x32_bf16 v[96:99], a[212:215], v[204:207], v[96:99]\n"
    "s_waitcnt lgkmcnt(2)\n"
    "v_exp_f32 v152, v152\n"
    "v_mfma_f32_16x16x32_bf16 v[100:103], a[216:219], v[168:171], v[100:103]\n"
    "v_mfma_f32_16x16x32_bf16 v[100:103], a[220:223], v[172:175], v[100:103]\n"
    "v_exp_f32 v153, v153\n"
    "v_mfma_f32_16x16x32_bf16 v[104:107], a[216:219], v[184:187], v[104:107]\n"
    "v_cvt_pk_bf16_f32 v196, v152, v153\n"
    "v_mfma_f32_16x16x32_bf16 v[104:107], a[220:223], v[188:191], v[104:107]\n"
    "v_exp_f32 v154, v154\n"
    "v_mfma_f32_16x16x32_bf16 v[108:111], a[216:219], v[200:203], v[108:111]\n"
    "s_waitcnt vmcnt(13)\n"
    "s_waitcnt vmcnt(18) lgkmcnt(0)\n"
    "ds_read_b128 v[228:231], v8 offset:2048\n"
    "v_mfma_f32_16x16x32_bf16 v[108:111], a[220:223], v[204:207], v[108:111]\n"
    "s_barrier\n"
    "v_exp_f32 v155, v155\n"
    "v_mfma_f32_16x16x32_bf16 v[112:115], a[96:99], a[0:3], v[208:211]\n"
    "s_add_u32 s63, s59, 98304\n"
    "ds_read_b128 v[220:223], v9 offset:2048\n"
    "v_cvt_pk_bf16_f32 v197, v154, v155\n"
    "v_mfma_f32_16x16x32_bf16 v[112:115], a[100:103], a[4:7], v[112:115]\n"
    "s_add_u32 m0, s60, 0\n"
    "v_exp_f32 v124, v124\n"
    "v_mfma_f32_16x16x32_bf16 v[112:115], a[104:107], a[8:11], v[112:115]\n"
    "buffer_load_dwordx4 v7, s[44:47], s63 offen nt lds\n"
    "v_mfma_f32_16x16x32_bf16 v[112:115], a[108:111], a[12:15], v[112:115]\n"
    "v_exp_f32 v125, v125\n"
    "v_mfma_f32_16x16x32_bf16 v[128:131], a[96:99], a[16:19], v[212:215]\n"
    "s_waitcnt lgkmcnt(1)\n"
    "v_cvt_pk_bf16_f32 v166, v124, v125\n"
    "v_lshlrev_b32 v246, 16, v228\n"
    "v_mfma_f32_16x16x32_bf16 v[128:131], a[100:103], a[20:23], v[128:131]\n"
    "v_exp_f32 v126, v126\n"
    "v_mfma_f32_16x16x32_bf16 v[128:131], a[104:107], a[24:27], v[128:131]\n"
    "v_and_b32 v228, 0xffff0000, v228\n"
    "v_mul_f32 v246, s56, v246\n"
    "v_mul_f32 v228, s56, v228\n"
    "v_cvt_pk_bf16_f32 v228, v246, v228\n"
    "v_accvgpr_write_b32 a56, v228\n"
    "v_lshlrev_b32 v246, 16, v229\n"
    "v_and_b32 v229, 0xffff0000, v229\n"
    "v_mul_f32 v246, s56, v246\n"
    "v_mul_f32 v229, s56, v229\n"
    "v_cvt_pk_bf16_f32 v229, v246, v229\n"
    "v_accvgpr_write_b32 a57, v229\n"
    "v_lshlrev_b32 v246, 16, v230\n"
    "v_and_b32 v230, 0xffff0000, v230\n"
    "v_mul_f32 v246, s56, v246\n"
    "v_mul_f32 v230, s56, v230\n"
    "v_cvt_pk_bf16_f32 v230, v246, v230\n"
    "v_accvgpr_write_b32 a58, v230\n"
    "v_lshlrev_b32 v246, 16, v231\n"
    "v_and_b32 v231, 0xffff0000, v231\n"
    "v_mul_f32 v246, s56, v246\n"
    "v_mul_f32 v231, s56, v231\n"
    "v_cvt_pk_bf16_f32 v231, v246, v231\n"
    "v_accvgpr_write_b32 a59, v231\n"
    "v_mfma_f32_16x16x32_bf16 v[128:131], a[108:111], a[28:31], v[128:131]\n"
    "s_waitcnt lgkmcnt(0)\n"
    "v_lshlrev_b32 v246, 16, v220\n"
    "v_and_b32 v220, 0xffff0000, v220\n"
    "v_mfma_f32_16x16x32_bf16 v[144:147], a[96:99], a[32:35], v[216:219]\n"
    "v_exp_f32 v127, v127\n"
    "v_mfma_f32_16x16x32_bf16 v[144:147], a[100:103], a[36:39], v[144:147]\n"
    "s_add_u32 s64, s59, 98432\n"
    "v_cvt_pk_bf16_f32 v167, v126, v127\n"
    "v_mul_f32 v246, s56, v246\n"
    "v_mfma_f32_16x16x32_bf16 v[144:147], a[104:107], a[40:43], v[144:147]\n"
    "s_add_u32 m0, s60, 1024\n"
    "v_exp_f32 v140, v140\n"
    "v_mfma_f32_16x16x32_bf16 v[144:147], a[108:111], a[44:47], v[144:147]\n"
    "buffer_load_dwordx4 v7, s[44:47], s64 offen nt lds\n"
    "v_mfma_f32_16x16x32_bf16 v[116:119], a[112:115], a[0:3], v[208:211]\n"
    "v_exp_f32 v141, v141\n"
    "v_mfma_f32_16x16x32_bf16 v[116:119], a[116:119], a[4:7], v[116:119]\n"
    "v_cvt_pk_bf16_f32 v182, v140, v141\n"
    "v_mul_f32 v220, s56, v220\n"
    "v_mfma_f32_16x16x32_bf16 v[116:119], a[120:123], a[8:11], v[116:119]\n"
    "v_exp_f32 v142, v142\n"
    "v_mfma_f32_16x16x32_bf16 v[116:119], a[124:127], a[12:15], v[116:119]\n"
    "v_cvt_pk_bf16_f32 v220, v246, v220\n"
    "v_accvgpr_write_b32 a60, v220\n"
    "v_lshlrev_b32 v246, 16, v221\n"
    "v_and_b32 v221, 0xffff0000, v221\n"
    "v_mfma_f32_16x16x32_bf16 v[132:135], a[112:115], a[16:19], v[212:215]\n"
    "v_exp_f32 v143, v143\n"
    "v_mul_f32 v246, s56, v246\n"
    "v_mul_f32 v221, s56, v221\n"
    "v_mfma_f32_16x16x32_bf16 v[132:135], a[116:119], a[20:23], v[132:135]\n"
    "s_add_u32 s65, s59, 100352\n"
    "ds_read_b64_tr_b16 a[222:223], v4 offset:30208\n"
    "v_cvt_pk_bf16_f32 v183, v142, v143\n"
    "v_cvt_pk_bf16_f32 v221, v246, v221\n"
    "v_accvgpr_write_b32 a61, v221\n"
    "v_mfma_f32_16x16x32_bf16 v[132:135], a[120:123], a[24:27], v[132:135]\n"
    "s_add_u32 m0, s60, 2048\n"
    "v_exp_f32 v156, v156\n"
    "v_lshlrev_b32 v246, 16, v222\n"
    "v_and_b32 v222, 0xffff0000, v222\n"
    "v_mfma_f32_16x16x32_bf16 v[132:135], a[124:127], a[28:31], v[132:135]\n"
    "buffer_load_dwordx4 v7, s[44:47], s65 offen nt lds\n"
    "v_mul_f32 v246, s56, v246\n"
    "v_mul_f32 v222, s56, v222\n"
    "v_mfma_f32_16x16x32_bf16 v[148:151], a[112:115], a[32:35], v[216:219]\n"
    "v_exp_f32 v157, v157\n"
    "v_cvt_pk_bf16_f32 v222, v246, v222\n"
    "v_accvgpr_write_b32 a62, v222\n"
    "v_mfma_f32_16x16x32_bf16 v[148:151], a[116:119], a[36:39], v[148:151]\n"
    "ds_read_b64_tr_b16 a[220:221], v4 offset:26112\n"
    "v_cvt_pk_bf16_f32 v198, v156, v157\n"
    "v_lshlrev_b32 v246, 16, v223\n"
    "v_and_b32 v223, 0xffff0000, v223\n"
    "v_mfma_f32_16x16x32_bf16 v[148:151], a[120:123], a[40:43], v[148:151]\n"
    "s_waitcnt vmcnt(14)\n"
    "ds_read_b128 v[224:227], v8 offset:4096\n"
    "v_exp_f32 v158, v158\n"
    "v_mul_f32 v246, s56, v246\n"
    "v_mul_f32 v223, s56, v223\n"
    "v_cvt_pk_bf16_f32 v223, v246, v223\n"
    "v_accvgpr_write_b32 a63, v223\n"
    "v_mfma_f32_16x16x32_bf16 v[148:151], a[124:127], a[44:47], v[148:151]\n"
    "ds_read_b64_tr_b16 a[160:161], v3 offset:16384\n"
    "ds_read_b64_tr_b16 a[162:163], v3 offset:20480\n"
    "v_mfma_f32_16x16x32_bf16 v[120:123], a[128:131], a[0:3], v[208:211]\n"
    "v_exp_f32 v159, v159\n"
    "v_mfma_f32_16x16x32_bf16 v[120:123], a[132:135], a[4:7], v[120:123]\n"
    "s_add_u32 s62, s59, 100480\n"
    "ds_read_b64_tr_b16 a[164:165], v3 offset:24576\n"
    "ds_read_b64_tr_b16 a[166:167], v3 offset:28672\n"
    "v_mfma_f32_16x16x32_bf16 v[120:123], a[136:139], a[8:11], v[120:123]\n"
    "s_add_u32 m0, s60, 3072\n"
    "v_exp_f32 v112, v112\n"
    "v_mfma_f32_16x16x32_bf16 v[120:123], a[140:143], a[12:15], v[120:123]\n"
    "buffer_load_dwordx4 v7, s[44:47], s62 offen nt lds\n"
    "v_mfma_f32_16x16x32_bf16 v[136:139], a[128:131], a[16:19], v[212:215]\n"
    "v_exp_f32 v113, v113\n"
    "v_mfma_f32_16x16x32_bf16 v[136:139], a[132:135], a[20:23], v[136:139]\n"
    "s_waitcnt lgkmcnt(4)\n"
    "ds_read_b64_tr_b16 a[168:169], v4 offset:16384\n"
    "v_cvt_pk_bf16_f32 v199, v158, v159\n"
    "v_lshlrev_b32 v246, 16, v224\n"
    "v_mfma_f32_16x16x32_bf16 v[136:139], a[136:139], a[24:27], v[136:139]\n"
    "ds_read_b64_tr_b16 a[170:171], v4 offset:20480\n"
    "v_cvt_pk_bf16_f32 v168, v112, v113\n"
    "v_and_b32 v224, 0xffff0000, v224\n"
    "v_mfma_f32_16x16x32_bf16 v[136:139], a[140:143], a[28:31], v[136:139]\n"
    "v_exp_f32 v114, v114\n"
    "v_mul_f32 v246, s56, v246\n"
    "v_mfma_f32_16x16x32_bf16 v[152:155], a[128:131], a[32:35], v[216:219]\n"
    "ds_read_b64_tr_b16 a[172:173], v4 offset:24576\n"
    "ds_read_b64_tr_b16 a[174:175], v4 offset:28672\n"
    "v_mul_f32 v224, s56, v224\n"
    "v_mfma_f32_16x16x32_bf16 v[152:155], a[132:135], a[36:39], v[152:155]\n"
    "v_exp_f32 v115, v115\n"
    "v_cvt_pk_bf16_f32 v224, v246, v224\n"
    "v_mfma_f32_16x16x32_bf16 v[152:155], a[136:139], a[40:43], v[152:155]\n"
    "s_add_u32 s63, s57, 16384\n"
    "ds_read_b64_tr_b16 a[176:177], v3 offset:16896\n"
    "v_cvt_pk_bf16_f32 v169, v114, v115\n"
    "v_accvgpr_write_b32 a64, v224\n"
    "v_lshlrev_b32 v246, 16, v225\n"
    "v_mfma_f32_16x16x32_bf16 v[152:155], a[140:143], a[44:47], v[152:155]\n"
    "s_add_u32 m0, s58, 16384\n"
    "v_exp_f32 v128, v128\n"
    "v_and_b32 v225, 0xffff0000, v225\n"
    "v_mfma_f32_16x16x32_bf16 v[124:127], a[144:147], a[0:3], v[208:211]\n"
    "buffer_load_dwordx4 v5, s[40:43], s63 offen nt lds\n"
    "v_mul_f32 v246, s56, v246\n"
    "v_mfma_f32_16x16x32_bf16 v[124:127], a[148:151], a[4:7], v[124:127]\n"
    "v_exp_f32 v129, v129\n"
    "v_mul_f32 v225, s56, v225\n"
    "v_mfma_f32_16x16x32_bf16 v[124:127], a[152:155], a[8:11], v[124:127]\n"
    "ds_read_b64_tr_b16 a[178:179], v3 offset:20992\n"
    "ds_read_b64_tr_b16 a[180:181], v3 offset:25088\n"
    "v_cvt_pk_bf16_f32 v225, v246, v225\n"
    "v_mfma_f32_16x16x32_bf16 v[124:127], a[156:159], a[12:15], v[124:127]\n"
    "v_exp_f32 v130, v130\n"
    "v_accvgpr_write_b32 a65, v225\n"
    "v_mfma_f32_16x16x32_bf16 v[140:143], a[144:147], a[16:19], v[212:215]\n"
    "ds_read_b64_tr_b16 a[182:183], v3 offset:29184\n"
    "v_cvt_pk_bf16_f32 v184, v128, v129\n"
    "v_lshlrev_b32 v246, 16, v226\n"
    "v_mfma_f32_16x16x32_bf16 v[140:143], a[148:151], a[20:23], v[140:143]\n"
    "v_exp_f32 v131, v131\n"
    "v_and_b32 v226, 0xffff0000, v226\n"
    "v_mfma_f32_16x16x32_bf16 v[140:143], a[152:155], a[24:27], v[140:143]\n"
    "s_add_u32 s64, s57, 18432\n"
    "ds_read_b64_tr_b16 a[184:185], v4 offset:16896\n"
    "ds_read_b64_tr_b16 a[186:187], v4 offset:20992\n"
    "v_mul_f32 v246, s56, v246\n"
    "v_mfma_f32_16x16x32_bf16 v[140:143], a[156:159], a[28:31], v[140:143]\n"
    "s_add_u32 m0, s58, 17408\n"
    "v_exp_f32 v144, v144\n"
    "v_mul_f32 v226, s56, v226\n"
    "v_mfma_f32_16x16x32_bf16 v[156:159], a[144:147], a[32:35], v[216:219]\n"
    "buffer_load_dwordx4 v6, s[40:43], s64 offen nt lds\n"
    "v_cvt_pk_bf16_f32 v226, v246, v226\n"
    "v_mfma_f32_16x16x32_bf16 v[156:159], a[148:151], a[36:39], v[156:159]\n"
    "v_exp_f32 v145, v145\n"
    "v_accvgpr_write_b32 a66, v226\n"
    "v_mfma_f32_16x16x32_bf16 v[156:159], a[152:155], a[40:43], v[156:159]\n"
    "ds_read_b64_tr_b16 a[188:189], v4 offset:25088\n"
    "v_cvt_pk_bf16_f32 v185, v130, v131\n"
    "v_lshlrev_b32 v246, 16, v227\n"
    "v_and_b32 v227, 0xffff0000, v227\n"
    "v_mfma_f32_16x16x32_bf16 v[156:159], a[156:159], a[44:47], v[156:159]\n"
    "v_exp_f32 v146, v146\n"
    "v_mul_f32 v246, s56, v246\n"
    "v_mfma_f32_16x16x32_bf16 a[224:227], a[236:239], v[160:163], a[224:227]\n"
    "ds_read_b64_tr_b16 a[190:191], v4 offset:29184\n"
    "v_cvt_pk_bf16_f32 v200, v144, v145\n"
    "v_mul_f32 v227, s56, v227\n"
    "v_mfma_f32_16x16x32_bf16 a[224:227], a[236:239], v[164:167], a[224:227]\n"
    "v_exp_f32 v147, v147\n"
    "v_cvt_pk_bf16_f32 v227, v246, v227\n"
    "v_mfma_f32_16x16x32_bf16 a[228:231], a[236:239], v[176:179], a[228:231]\n"
    "ds_read_b64_tr_b16 a[192:193], v3 offset:17408\n"
    "ds_read_b64_tr_b16 a[194:195], v3 offset:21504\n"
    "v_accvgpr_write_b32 a67, v227\n"
    "v_mfma_f32_16x16x32_bf16 a[228:231], a[236:239], v[180:183], a[228:231]\n"
    "s_add_u32 s65, s57, 20480\n"
    "v_exp_f32 v116, v116\n"
    "v_mfma_f32_16x16x32_bf16 a[232:235], a[236:239], v[192:195], a[232:235]\n"
    "s_add_u32 m0, s58, 18432\n"
    "ds_read_b128 a[96:99], v1 offset:32768\n"
    "v_cvt_pk_bf16_f32 v201, v146, v147\n"
    "v_mfma_f32_16x16x32_bf16 a[232:235], a[236:239], v[196:199], a[232:235]\n"
    "s_waitcnt lgkmcnt(4)\n"
    "v_exp_f32 v117, v117\n"
    "v_mfma_f32_16x16x32_bf16 v[16:19], a[160:163], v[160:163], v[16:19]\n"
    "buffer_load_dwordx4 v5, s[40:43], s65 offen nt lds\n"
    "ds_read_b128 v[228:231], v9 offset:4096\n"
    "v_mfma_f32_16x16x32_bf16 v[16:19], a[164:167], v[164:167], v[16:19]\n"
    "ds_read_b64_tr_b16 a[196:197], v3 offset:25600\n"
    "ds_read_b64_tr_b16 a[198:199], v3 offset:29696\n"
    "v_mfma_f32_16x16x32_bf16 v[20:23], a[160:163], v[176:179], v[20:23]\n"
    "v_exp_f32 v118, v118\n"
    "v_mfma_f32_16x16x32_bf16 v[20:23], a[164:167], v[180:183], v[20:23]\n"
    "ds_read_b128 a[100:103], v2 offset:32768\n"
    "v_cvt_pk_bf16_f32 v170, v116, v117\n"
    "v_mfma_f32_16x16x32_bf16 v[24:27], a[160:163], v[192:195], v[24:27]\n"
    "s_add_u32 s62, s57, 22528\n"
    "v_exp_f32 v119, v119\n"
    "v_mfma_f32_16x16x32_bf16 v[24:27], a[164:167], v[196:199], v[24:27]\n"
    "s_waitcnt lgkmcnt(3)\n"
    "ds_read_b128 a[104:107], v1 offset:40960\n"
    "v_cvt_pk_bf16_f32 v171, v118, v119\n"
    "v_lshlrev_b32 v246, 16, v228\n"
    "v_and_b32 v228, 0xffff0000, v228\n"
    "v_mfma_f32_16x16x32_bf16 v[28:31], a[168:171], v[160:163], v[28:31]\n"
    "s_add_u32 m0, s58, 19456\n"
    "v_exp_f32 v132, v132\n"
    "v_mul_f32 v246, s56, v246\n"
    "v_mfma_f32_16x16x32_bf16 v[28:31], a[172:175], v[164:167], v[28:31]\n"
    "buffer_load_dwordx4 v6, s[40:43], s62 offen nt lds\n"
    "ds_read_b128 a[156:159], v2 offset:47104\n"
    "v_mul_f32 v228, s56, v228\n"
    "v_mfma_f32_16x16x32_bf16 v[32:35], a[168:171], v[176:179], v[32:35]\n"
    "ds_read_b128 a[148:151], v2 offset:38912\n"
    "v_exp_f32 v133, v133\n"
    "v_cvt_pk_bf16_f32 v228, v246, v228\n"
    "v_mfma_f32_16x16x32_bf16 v[32:35], a[172:175], v[180:183], v[32:35]\n"
    "ds_read_b64_tr_b16 a[200:201], v4 offset:17408\n"
    "ds_read_b64_tr_b16 a[202:203], v4 offset:21504\n"
    "v_accvgpr_write_b32 a68, v228\n"
    "v_mfma_f32_16x16x32_bf16 v[36:39], a[168:171], v[192:195], v[36:39]\n"
    "v_exp_f32 v134, v134\n"
    "v_lshlrev_b32 v246, 16, v229\n"
    "v_mfma_f32_16x16x32_bf16 v[36:39], a[172:175], v[196:199], v[36:39]\n"
    "ds_read_b128 a[108:111], v2 offset:40960\n"
    "v_cvt_pk_bf16_f32 v186, v132, v133\n"
    "v_and_b32 v229, 0xffff0000, v229\n"
    "v_mfma_f32_16x16x32_bf16 v[40:43], a[176:179], v[160:163], v[40:43]\n"
    "v_exp_f32 v135, v135\n"
    "v_mul_f32 v246, s56, v246\n"
    "v_mfma_f32_16x16x32_bf16 v[40:43], a[180:183], v[164:167], v[40:43]\n"
    "ds_read_b64_tr_b16 a[204:205], v4 offset:25600\n"
    "ds_read_b64_tr_b16 a[206:207], v4 offset:29696\n"
    "v_mul_f32 v229, s56, v229\n"
    "v_mfma_f32_16x16x32_bf16 v[44:47], a[176:179], v[176:179], v[44:47]\n"
    "v_exp_f32 v148, v148\n"
    "v_cvt_pk_bf16_f32 v229, v246, v229\n"
    "v_mfma_f32_16x16x32_bf16 v[44:47], a[180:183], v[180:183], v[44:47]\n"
    "s_add_u32 s63, s61, 59392\n"
    "ds_read_b128 a[112:115], v1 offset:34816\n"
    "v_cvt_pk_bf16_f32 v187, v134, v135\n"
    "v_accvgpr_write_b32 a69, v229\n"
    "v_mfma_f32_16x16x32_bf16 v[48:51], a[176:179], v[192:195], v[48:51]\n"
    "s_add_u32 m0, s78, 9216\n"
    "v_exp_f32 v149, v149\n"
    "v_lshlrev_b32 v246, 16, v230\n"
    "v_and_b32 v230, 0xffff0000, v230\n"
    "v_mfma_f32_16x16x32_bf16 v[48:51], a[180:183], v[196:199], v[48:51]\n"
    "buffer_load_dwordx4 v6, s[48:51], s63 offen nt lds\n"
    "ds_read_b128 a[140:143], v2 offset:45056\n"
    "v_mul_f32 v246, s56, v246\n"
    "v_mfma_f32_16x16x32_bf16 v[52:55], a[184:187], v[160:163], v[52:55]\n"
    "v_exp_f32 v150, v150\n"
    "v_mul_f32 v230, s56, v230\n"
    "v_mfma_f32_16x16x32_bf16 v[52:55], a[188:191], v[164:167], v[52:55]\n"
    "ds_read_b128 a[116:119], v2 offset:34816\n"
    "v_cvt_pk_bf16_f32 v202, v148, v149\n"
    "v_cvt_pk_bf16_f32 v230, v246, v230\n"
    "v_mfma_f32_16x16x32_bf16 v[56:59], a[184:187], v[176:179], v[56:59]\n"
    "v_exp_f32 v151, v151\n"
    "v_accvgpr_write_b32 a70, v230\n"
    "v_mfma_f32_16x16x32_bf16 v[56:59], a[188:191], v[180:183], v[56:59]\n"
    "ds_read_b64_tr_b16 a[208:209], v3 offset:17920\n"
    "ds_read_b64_tr_b16 a[210:211], v3 offset:22016\n"
    "v_lshlrev_b32 v246, 16, v231\n"
    "v_mfma_f32_16x16x32_bf16 v[60:63], a[184:187], v[192:195], v[60:63]\n"
    "s_add_u32 s64, s61, 57472\n"
    "v_exp_f32 v120, v120\n"
    "v_and_b32 v231, 0xffff0000, v231\n"
    "v_mfma_f32_16x16x32_bf16 v[60:63], a[188:191], v[196:199], v[60:63]\n"
    "s_add_u32 m0, s78, 10240\n"
    "ds_read_b128 a[120:123], v1 offset:43008\n"
    "v_cvt_pk_bf16_f32 v203, v150, v151\n"
    "v_mul_f32 v246, s56, v246\n"
    "v_mfma_f32_16x16x32_bf16 v[64:67], a[192:195], v[160:163], v[64:67]\n"
    "s_waitcnt lgkmcnt(4)\n"
    "v_exp_f32 v121, v121\n"
    "v_mul_f32 v231, s56, v231\n"
    "v_mfma_f32_16x16x32_bf16 v[64:67], a[196:199], v[164:167], v[64:67]\n"
    "buffer_load_dwordx4 v5, s[48:51], s64 offen nt lds\n"
    "ds_read_b128 a[144:147], v1 offset:38912\n"
    "v_cvt_pk_bf16_f32 v231, v246, v231\n"
    "v_mfma_f32_16x16x32_bf16 v[68:71], a[192:195], v[176:179], v[68:71]\n"
    "ds_read_b64_tr_b16 a[212:213], v3 offset:26112\n"
    "ds_read_b64_tr_b16 a[214:215], v3 offset:30208\n"
    "v_accvgpr_write_b32 a71, v231\n"
    "v_mfma_f32_16x16x32_bf16 v[68:71], a[196:199], v[180:183], v[68:71]\n"
    "v_exp_f32 v122, v122\n"
    "v_mfma_f32_16x16x32_bf16 v[72:75], a[192:195], v[192:195], v[72:75]\n"
    "ds_read_b128 a[124:127], v2 offset:43008\n"
    "v_cvt_pk_bf16_f32 v172, v120, v121\n"
    "v_mfma_f32_16x16x32_bf16 v[72:75], a[196:199], v[196:199], v[72:75]\n"
    "v_exp_f32 v123, v123\n"
    "v_mfma_f32_16x16x32_bf16 v[76:79], a[200:203], v[160:163], v[76:79]\n"
    "ds_read_b128 a[128:131], v1 offset:36864\n"
    "v_cvt_pk_bf16_f32 v173, v122, v123\n"
    "v_mfma_f32_16x16x32_bf16 v[76:79], a[204:207], v[164:167], v[76:79]\n"
    "v_exp_f32 v136, v136\n"
    "v_mfma_f32_16x16x32_bf16 v[80:83], a[200:203], v[176:179], v[80:83]\n"
    "s_add_u32 s65, s61, 59520\n"
    "ds_read_b64_tr_b16 a[216:217], v4 offset:17920\n"
    "ds_read_b64_tr_b16 a[218:219], v4 offset:22016\n"
    "v_mfma_f32_16x16x32_bf16 v[80:83], a[204:207], v[180:183], v[80:83]\n"
    "s_add_u32 m0, s78, 11264\n"
    "v_exp_f32 v137, v137\n"
    "v_mfma_f32_16x16x32_bf16 v[84:87], a[200:203], v[192:195], v[84:87]\n"
    "buffer_load_dwordx4 v6, s[48:51], s65 offen nt lds\n"
    "ds_read_b128 a[152:155], v1 offset:47104\n"
    "v_mfma_f32_16x16x32_bf16 v[84:87], a[204:207], v[196:199], v[84:87]\n"
    "s_waitcnt lgkmcnt(4)\n"
    "v_exp_f32 v138, v138\n"
    "v_mfma_f32_16x16x32_bf16 v[88:91], a[208:211], v[160:163], v[88:91]\n"
    "ds_read_b128 a[132:135], v2 offset:36864\n"
    "v_cvt_pk_bf16_f32 v188, v136, v137\n"
    "v_mfma_f32_16x16x32_bf16 v[88:91], a[212:215], v[164:167], v[88:91]\n"
    "v_exp_f32 v139, v139\n"
    "v_mfma_f32_16x16x32_bf16 v[92:95], a[208:211], v[176:179], v[92:95]\n"
    "ds_read_b128 a[136:139], v1 offset:45056\n"
    "v_cvt_pk_bf16_f32 v189, v138, v139\n"
    "v_mfma_f32_16x16x32_bf16 v[92:95], a[212:215], v[180:183], v[92:95]\n"
    "v_exp_f32 v152, v152\n"
    "v_mfma_f32_16x16x32_bf16 v[96:99], a[208:211], v[192:195], v[96:99]\n"
    "v_mfma_f32_16x16x32_bf16 v[96:99], a[212:215], v[196:199], v[96:99]\n"
    "s_waitcnt lgkmcnt(2)\n"
    "v_exp_f32 v153, v153\n"
    "v_mfma_f32_16x16x32_bf16 v[100:103], a[216:219], v[160:163], v[100:103]\n"
    "v_cvt_pk_bf16_f32 v204, v152, v153\n"
    "v_mfma_f32_16x16x32_bf16 v[100:103], a[220:223], v[164:167], v[100:103]\n"
    "v_exp_f32 v154, v154\n"
    "v_mfma_f32_16x16x32_bf16 v[104:107], a[216:219], v[176:179], v[104:107]\n"
    "v_mfma_f32_16x16x32_bf16 v[104:107], a[220:223], v[180:183], v[104:107]\n"
    "v_exp_f32 v155, v155\n"
    "v_mfma_f32_16x16x32_bf16 v[108:111], a[216:219], v[192:195], v[108:111]\n"
    "s_waitcnt vmcnt(18) lgkmcnt(0)\n"
    "v_cvt_pk_bf16_f32 v205, v154, v155\n"
    "v_mfma_f32_16x16x32_bf16 v[108:111], a[220:223], v[196:199], v[108:111]\n"
    "s_barrier\n"
    "v_exp_f32 v124, v124\n"
    "v_mfma_f32_16x16x32_bf16 v[112:115], a[96:99], a[0:3], v[208:211]\n"
    "v_mfma_f32_16x16x32_bf16 v[112:115], a[100:103], a[4:7], v[112:115]\n"
    "s_add_u32 s62, s59, 114688\n"
    "v_exp_f32 v125, v125\n"
    "v_mfma_f32_16x16x32_bf16 v[112:115], a[104:107], a[8:11], v[112:115]\n"
    "s_add_u32 m0, s60, 16384\n"
    "v_cvt_pk_bf16_f32 v174, v124, v125\n"
    "v_mfma_f32_16x16x32_bf16 v[112:115], a[108:111], a[12:15], v[112:115]\n"
    "buffer_load_dwordx4 v7, s[44:47], s62 offen nt lds\n"
    "v_mfma_f32_16x16x32_bf16 v[128:131], a[96:99], a[16:19], v[212:215]\n"
    "v_exp_f32 v126, v126\n"
    "v_mfma_f32_16x16x32_bf16 v[128:131], a[100:103], a[20:23], v[128:131]\n"
    "s_waitcnt vmcnt(13)\n"
    "ds_read_b128 v[220:223], v8 offset:6144\n"
    "v_mfma_f32_16x16x32_bf16 v[128:131], a[104:107], a[24:27], v[128:131]\n"
    "v_exp_f32 v127, v127\n"
    "v_mfma_f32_16x16x32_bf16 v[128:131], a[108:111], a[28:31], v[128:131]\n"
    "v_cvt_pk_bf16_f32 v175, v126, v127\n"
    "v_mfma_f32_16x16x32_bf16 v[144:147], a[96:99], a[32:35], v[216:219]\n"
    "v_exp_f32 v140, v140\n"
    "v_mfma_f32_16x16x32_bf16 v[144:147], a[100:103], a[36:39], v[144:147]\n"
    "ds_read_b128 v[224:227], v9 offset:6144\n"
    "v_mfma_f32_16x16x32_bf16 v[144:147], a[104:107], a[40:43], v[144:147]\n"
    "s_add_u32 s63, s59, 114816\n"
    "v_exp_f32 v141, v141\n"
    "v_mfma_f32_16x16x32_bf16 v[144:147], a[108:111], a[44:47], v[144:147]\n"
    "s_waitcnt lgkmcnt(1)\n"
    "v_cvt_pk_bf16_f32 v190, v140, v141\n"
    "v_lshlrev_b32 v246, 16, v220\n"
    "v_and_b32 v220, 0xffff0000, v220\n"
    "v_mul_f32 v246, s56, v246\n"
    "v_mfma_f32_16x16x32_bf16 v[116:119], a[112:115], a[0:3], v[208:211]\n"
    "s_add_u32 m0, s60, 17408\n"
    "v_exp_f32 v142, v142\n"
    "v_mul_f32 v220, s56, v220\n"
    "v_cvt_pk_bf16_f32 v220, v246, v220\n"
    "v_mfma_f32_16x16x32_bf16 v[116:119], a[116:119], a[4:7], v[116:119]\n"
    "buffer_load_dwordx4 v7, s[44:47], s63 offen nt lds\n"
    "v_accvgpr_write_b32 a72, v220\n"
    "v_lshlrev_b32 v246, 16, v221\n"
    "v_mfma_f32_16x16x32_bf16 v[116:119], a[120:123], a[8:11], v[116:119]\n"
    "v_exp_f32 v143, v143\n"
    "v_and_b32 v221, 0xffff0000, v221\n"
    "v_mul_f32 v246, s56, v246\n"
    "v_mul_f32 v221, s56, v221\n"
    "v_cvt_pk_bf16_f32 v221, v246, v221\n"
    "v_accvgpr_write_b32 a73, v221\n"
    "v_lshlrev_b32 v246, 16, v222\n"
    "v_and_b32 v222, 0xffff0000, v222\n"
    "v_mul_f32 v246, s56, v246\n"
    "v_mul_f32 v222, s56, v222\n"
    "v_cvt_pk_bf16_f32 v222, v246, v222\n"
    "v_accvgpr_write_b32 a74, v222\n"
    "v_lshlrev_b32 v246, 16, v223\n"
    "v_and_b32 v223, 0xffff0000, v223\n"
    "v_mul_f32 v246, s56, v246\n"
    "v_mul_f32 v223, s56, v223\n"
    "v_cvt_pk_bf16_f32 v223, v246, v223\n"
    "v_accvgpr_write_b32 a75, v223\n"
    "v_mfma_f32_16x16x32_bf16 v[116:119], a[124:127], a[12:15], v[116:119]\n"
    "s_waitcnt lgkmcnt(0)\n"
    "v_cvt_pk_bf16_f32 v191, v142, v143\n"
    "v_lshlrev_b32 v246, 16, v224\n"
    "v_mfma_f32_16x16x32_bf16 v[132:135], a[112:115], a[16:19], v[212:215]\n"
    "v_exp_f32 v156, v156\n"
    "v_mfma_f32_16x16x32_bf16 v[132:135], a[116:119], a[20:23], v[132:135]\n"
    "v_and_b32 v224, 0xffff0000, v224\n"
    "v_mul_f32 v246, s56, v246\n"
    "v_mfma_f32_16x16x32_bf16 v[132:135], a[120:123], a[24:27], v[132:135]\n"
    "v_exp_f32 v157, v157\n"
    "v_mfma_f32_16x16x32_bf16 v[132:135], a[124:127], a[28:31], v[132:135]\n"
    "v_cvt_pk_bf16_f32 v206, v156, v157\n"
    "v_mul_f32 v224, s56, v224\n"
    "v_cvt_pk_bf16_f32 v224, v246, v224\n"
    "v_mfma_f32_16x16x32_bf16 v[148:151], a[112:115], a[32:35], v[216:219]\n"
    "v_exp_f32 v158, v158\n"
    "v_accvgpr_write_b32 a76, v224\n"
    "v_lshlrev_b32 v246, 16, v225\n"
    "v_mfma_f32_16x16x32_bf16 v[148:151], a[116:119], a[36:39], v[148:151]\n"
    "ds_read_b64_tr_b16 a[164:165], v3 offset:40960\n"
    "ds_read_b64_tr_b16 a[166:167], v3 offset:45056\n"
    "v_and_b32 v225, 0xffff0000, v225\n"
    "v_mfma_f32_16x16x32_bf16 v[148:151], a[120:123], a[40:43], v[148:151]\n"
    "v_exp_f32 v159, v159\n"
    "v_mul_f32 v246, s56, v246\n"
    "v_mfma_f32_16x16x32_bf16 v[148:151], a[124:127], a[44:47], v[148:151]\n"
    "s_add_u32 s64, s59, 116736\n"
    "ds_read_b64_tr_b16 a[160:161], v3 offset:32768\n"
    "ds_read_b64_tr_b16 a[162:163], v3 offset:36864\n"
    "v_mul_f32 v225, s56, v225\n"
    "v_mfma_f32_16x16x32_bf16 v[120:123], a[128:131], a[0:3], v[208:211]\n"
    "s_add_u32 m0, s60, 18432\n"
    "v_exp_f32 v112, v112\n"
    "v_cvt_pk_bf16_f32 v225, v246, v225\n"
    "v_mfma_f32_16x16x32_bf16 v[120:123], a[132:135], a[4:7], v[120:123]\n"
    "buffer_load_dwordx4 v7, s[44:47], s64 offen nt lds\n"
    "v_accvgpr_write_b32 a77, v225\n"
    "v_mfma_f32_16x16x32_bf16 v[120:123], a[136:139], a[8:11], v[120:123]\n"
    "v_exp_f32 v113, v113\n"
    "v_lshlrev_b32 v246, 16, v226\n"
    "v_mfma_f32_16x16x32_bf16 v[120:123], a[140:143], a[12:15], v[120:123]\n"
    "v_cvt_pk_bf16_f32 v207, v158, v159\n"
    "v_cvt_pk_bf16_f32 v160, v112, v113\n"
    "v_and_b32 v226, 0xffff0000, v226\n"
    "v_mfma_f32_16x16x32_bf16 v[136:139], a[128:131], a[16:19], v[212:215]\n"
    "ds_read_b64_tr_b16 a[168:169], v4 offset:32768\n"
    "ds_read_b64_tr_b16 a[170:171], v4 offset:36864\n"
    "v_mul_f32 v246, s56, v246\n"
    "v_mfma_f32_16x16x32_bf16 v[136:139], a[132:135], a[20:23], v[136:139]\n"
    "v_exp_f32 v114, v114\n"
    "v_mul_f32 v226, s56, v226\n"
    "v_mfma_f32_16x16x32_bf16 v[136:139], a[136:139], a[24:27], v[136:139]\n"
    "ds_read_b64_tr_b16 a[172:173], v4 offset:40960\n"
    "ds_read_b64_tr_b16 a[174:175], v4 offset:45056\n"
    "v_cvt_pk_bf16_f32 v226, v246, v226\n"
    "v_mfma_f32_16x16x32_bf16 v[136:139], a[140:143], a[28:31], v[136:139]\n"
    "v_exp_f32 v115, v115\n"
    "v_accvgpr_write_b32 a78, v226\n"
    "v_mfma_f32_16x16x32_bf16 v[152:155], a[128:131], a[32:35], v[216:219]\n"
    "ds_read_b64_tr_b16 a[182:183], v3 offset:45568\n"
    "v_cvt_pk_bf16_f32 v161, v114, v115\n"
    "v_lshlrev_b32 v246, 16, v227\n"
    "v_and_b32 v227, 0xffff0000, v227\n"
    "v_mfma_f32_16x16x32_bf16 v[152:155], a[132:135], a[36:39], v[152:155]\n"
    "v_exp_f32 v128, v128\n"
    "v_mul_f32 v246, s56, v246\n"
    "v_mfma_f32_16x16x32_bf16 v[152:155], a[136:139], a[40:43], v[152:155]\n"
    "ds_read_b64_tr_b16 a[176:177], v3 offset:33280\n"
    "ds_read_b64_tr_b16 a[178:179], v3 offset:37376\n"
    "v_mul_f32 v227, s56, v227\n"
    "v_mfma_f32_16x16x32_bf16 v[152:155], a[140:143], a[44:47], v[152:155]\n"
    "v_exp_f32 v129, v129\n"
    "v_cvt_pk_bf16_f32 v227, v246, v227\n"
    "v_mfma_f32_16x16x32_bf16 v[124:127], a[144:147], a[0:3], v[208:211]\n"
    "s_add_u32 s65, s59, 116864\n"
    "ds_read_b64_tr_b16 a[180:181], v3 offset:41472\n"
    "v_cvt_pk_bf16_f32 v176, v128, v129\n"
    "v_accvgpr_write_b32 a79, v227\n"
    "v_mfma_f32_16x16x32_bf16 v[124:127], a[148:151], a[4:7], v[124:127]\n"
    "s_add_u32 m0, s60, 19456\n"
    "v_exp_f32 v130, v130\n"
    "v_mfma_f32_16x16x32_bf16 v[124:127], a[152:155], a[8:11], v[124:127]\n"
    "Lpg3%=:\n"
    "s_cbranch_scc1 Lpg7%=\n"
    "buffer_load_dwordx4 v7, s[44:47], s65 offen nt lds\n"
    "v_mfma_f32_16x16x32_bf16 v[124:127], a[156:159], a[12:15], v[124:127]\n"
    "v_exp_f32 v131, v131\n"
    "v_mfma_f32_16x16x32_bf16 v[140:143], a[144:147], a[16:19], v[212:215]\n"
    "ds_read_b64_tr_b16 a[184:185], v4 offset:33280\n"
    "v_cvt_pk_bf16_f32 v177, v130, v131\n"
    "v_mfma_f32_16x16x32_bf16 v[140:143], a[148:151], a[20:23], v[140:143]\n"
    "v_exp_f32 v144, v144\n"
    "v_mfma_f32_16x16x32_bf16 v[140:143], a[152:155], a[24:27], v[140:143]\n"
    "ds_read_b64_tr_b16 a[186:187], v4 offset:37376\n"
    "ds_read_b64_tr_b16 a[188:189], v4 offset:41472\n"
    "v_mfma_f32_16x16x32_bf16 v[140:143], a[156:159], a[28:31], v[140:143]\n"
    "v_exp_f32 v145, v145\n"
    "v_mfma_f32_16x16x32_bf16 v[156:159], a[144:147], a[32:35], v[216:219]\n"
    "ds_read_b64_tr_b16 a[190:191], v4 offset:45568\n"
    "v_cvt_pk_bf16_f32 v192, v144, v145\n"
    "v_mfma_f32_16x16x32_bf16 v[156:159], a[148:151], a[36:39], v[156:159]\n"
    "v_exp_f32 v146, v146\n"
    "v_mfma_f32_16x16x32_bf16 v[156:159], a[152:155], a[40:43], v[156:159]\n"
    "ds_read_b64_tr_b16 a[192:193], v3 offset:33792\n"
    "ds_read_b64_tr_b16 a[194:195], v3 offset:37888\n"
    "v_mfma_f32_16x16x32_bf16 v[156:159], a[156:159], a[44:47], v[156:159]\n"
    "v_exp_f32 v147, v147\n"
    "v_mfma_f32_16x16x32_bf16 a[224:227], a[236:239], v[168:171], a[224:227]\n"
    "s_add_u32 s62, s57, 32768\n"
    "ds_read_b128 a[96:99], v1 offset:49152\n"
    "v_cvt_pk_bf16_f32 v193, v146, v147\n"
    "v_mfma_f32_16x16x32_bf16 a[224:227], a[236:239], v[172:175], a[224:227]\n"
    "s_add_u32 m0, s58, 32768\n"
    "v_exp_f32 v116, v116\n"
    "v_mfma_f32_16x16x32_bf16 a[228:231], a[236:239], v[184:187], a[228:231]\n"
    "buffer_load_dwordx4 v5, s[40:43], s62 offen nt lds\n"
    "v_mfma_f32_16x16x32_bf16 a[228:231], a[236:239], v[188:191], a[228:231]\n"
    "ds_read_b64_tr_b16 a[196:197], v3 offset:41984\n"
    "ds_read_b64_tr_b16 a[198:199], v3 offset:46080\n"
    "v_mfma_f32_16x16x32_bf16 a[232:235], a[236:239], v[200:203], a[232:235]\n"
    "v_exp_f32 v117, v117\n"
    "v_mfma_f32_16x16x32_bf16 a[232:235], a[236:239], v[204:207], a[232:235]\n"
    "s_waitcnt lgkmcnt(3)\n"
    "ds_read_b128 a[100:103], v2 offset:49152\n"
    "v_cvt_pk_bf16_f32 v162, v116, v117\n"
    "v_mfma_f32_16x16x32_bf16 v[16:19], a[160:163], v[168:171], v[16:19]\n"
    "v_exp_f32 v118, v118\n"
    "v_mfma_f32_16x16x32_bf16 v[16:19], a[164:167], v[172:175], v[16:19]\n"
    "ds_read_b128 a[104:107], v1 offset:57344\n"
    "v_mfma_f32_16x16x32_bf16 v[20:23], a[160:163], v[184:187], v[20:23]\n"
    "v_exp_f32 v119, v119\n"
    "v_mfma_f32_16x16x32_bf16 v[20:23], a[164:167], v[188:191], v[20:23]\n"
    "ds_read_b64_tr_b16 a[200:201], v4 offset:33792\n"
    "ds_read_b64_tr_b16 a[202:203], v4 offset:37888\n"
    "v_mfma_f32_16x16x32_bf16 v[24:27], a[160:163], v[200:203], v[24:27]\n"
    "v_exp_f32 v132, v132\n"
    "v_mfma_f32_16x16x32_bf16 v[24:27], a[164:167], v[204:207], v[24:27]\n"
    "s_add_u32 s63, s57, 34816\n"
    "ds_read_b128 a[108:111], v2 offset:57344\n"
    "v_cvt_pk_bf16_f32 v163, v118, v119\n"
    "v_mfma_f32_16x16x32_bf16 v[28:31], a[168:171], v[168:171], v[28:31]\n"
    "s_add_u32 m0, s58, 33792\n"
    "v_exp_f32 v133, v133\n"
    "v_mfma_f32_16x16x32_bf16 v[28:31], a[172:175], v[172:175], v[28:31]\n"
    "buffer_load_dwordx4 v6, s[40:43], s63 offen nt lds\n"
    "v_mfma_f32_16x16x32_bf16 v[32:35], a[168:171], v[184:187], v[32:35]\n"
    "v_exp_f32 v134, v134\n"
    "v_mfma_f32_16x16x32_bf16 v[32:35], a[172:175], v[188:191], v[32:35]\n"
    "ds_read_b64_tr_b16 a[204:205], v4 offset:41984\n"
    "ds_read_b64_tr_b16 a[206:207], v4 offset:46080\n"
    "v_mfma_f32_16x16x32_bf16 v[36:39], a[168:171], v[200:203], v[36:39]\n"
    "v_exp_f32 v135, v135\n"
    "v_mfma_f32_16x16x32_bf16 v[36:39], a[172:175], v[204:207], v[36:39]\n"
    "ds_read_b128 a[112:115], v1 offset:51200\n"
    "v_cvt_pk_bf16_f32 v178, v132, v133\n"
    "v_mfma_f32_16x16x32_bf16 v[40:43], a[176:179], v[168:171], v[40:43]\n"
    "v_exp_f32 v148, v148\n"
    "v_mfma_f32_16x16x32_bf16 v[40:43], a[180:183], v[172:175], v[40:43]\n"
    "ds_read_b128 a[116:119], v2 offset:51200\n"
    "v_cvt_pk_bf16_f32 v179, v134, v135\n"
    "v_mfma_f32_16x16x32_bf16 v[44:47], a[176:179], v[184:187], v[44:47]\n"
    "v_exp_f32 v149, v149\n"
    "v_mfma_f32_16x16x32_bf16 v[44:47], a[180:183], v[188:191], v[44:47]\n"
    "ds_read_b64_tr_b16 a[208:209], v3 offset:34304\n"
    "ds_read_b64_tr_b16 a[210:211], v3 offset:38400\n"
    "v_mfma_f32_16x16x32_bf16 v[48:51], a[176:179], v[200:203], v[48:51]\n"
    "v_exp_f32 v150, v150\n"
    "v_mfma_f32_16x16x32_bf16 v[48:51], a[180:183], v[204:207], v[48:51]\n"
    "s_add_u32 s64, s57, 36864\n"
    "ds_read_b128 a[120:123], v1 offset:59392\n"
    "v_cvt_pk_bf16_f32 v194, v148, v149\n"
    "v_mfma_f32_16x16x32_bf16 v[52:55], a[184:187], v[168:171], v[52:55]\n"
    "s_add_u32 m0, s58, 34816\n"
    "v_exp_f32 v151, v151\n"
    "v_mfma_f32_16x16x32_bf16 v[52:55], a[188:191], v[172:175], v[52:55]\n"
    "buffer_load_dwordx4 v5, s[40:43], s64 offen nt lds\n"
    "ds_read_b128 a[152:155], v1 offset:63488\n"
    "v_mfma_f32_16x16x32_bf16 v[56:59], a[184:187], v[184:187], v[56:59]\n"
    "ds_read_b128 a[148:151], v2 offset:55296\n"
    "v_exp_f32 v120, v120\n"
    "v_mfma_f32_16x16x32_bf16 v[56:59], a[188:191], v[188:191], v[56:59]\n"
    "ds_read_b64_tr_b16 a[212:213], v3 offset:42496\n"
    "ds_read_b64_tr_b16 a[214:215], v3 offset:46592\n"
    "v_mfma_f32_16x16x32_bf16 v[60:63], a[184:187], v[200:203], v[60:63]\n"
    "ds_read_b64_tr_b16 a[216:217], v4 offset:34304\n"
    "ds_read_b64_tr_b16 a[218:219], v4 offset:38400\n"
    "v_mfma_f32_16x16x32_bf16 v[60:63], a[188:191], v[204:207], v[60:63]\n"
    "v_exp_f32 v121, v121\n"
    "v_mfma_f32_16x16x32_bf16 v[64:67], a[192:195], v[168:171], v[64:67]\n"
    "s_waitcnt lgkmcnt(6)\n"
    "ds_read_b128 a[124:127], v2 offset:59392\n"
    "v_cvt_pk_bf16_f32 v195, v150, v151\n"
    "v_mfma_f32_16x16x32_bf16 v[64:67], a[196:199], v[172:175], v[64:67]\n"
    "v_exp_f32 v122, v122\n"
    "v_mfma_f32_16x16x32_bf16 v[68:71], a[192:195], v[184:187], v[68:71]\n"
    "ds_read_b128 a[128:131], v1 offset:53248\n"
    "v_cvt_pk_bf16_f32 v164, v120, v121\n"
    "v_mfma_f32_16x16x32_bf16 v[68:71], a[196:199], v[188:191], v[68:71]\n"
    "v_exp_f32 v123, v123\n"
    "v_mfma_f32_16x16x32_bf16 v[72:75], a[192:195], v[200:203], v[72:75]\n"
    "s_add_u32 s65, s57, 38912\n"
    "ds_read_b128 a[132:135], v2 offset:53248\n"
    "v_cvt_pk_bf16_f32 v165, v122, v123\n"
    "v_mfma_f32_16x16x32_bf16 v[72:75], a[196:199], v[204:207], v[72:75]\n"
    "s_add_u32 m0, s58, 35840\n"
    "v_exp_f32 v136, v136\n"
    "v_mfma_f32_16x16x32_bf16 v[76:79], a[200:203], v[168:171], v[76:79]\n"
    "buffer_load_dwordx4 v6, s[40:43], s65 offen nt lds\n"
    "ds_read_b128 a[156:159], v2 offset:63488\n"
    "v_mfma_f32_16x16x32_bf16 v[76:79], a[204:207], v[172:175], v[76:79]\n"
    "v_exp_f32 v137, v137\n"
    "v_mfma_f32_16x16x32_bf16 v[80:83], a[200:203], v[184:187], v[80:83]\n"
    "ds_read_b64_tr_b16 a[220:221], v4 offset:42496\n"
    "ds_read_b64_tr_b16 a[222:223], v4 offset:46592\n"
    "v_mfma_f32_16x16x32_bf16 v[80:83], a[204:207], v[188:191], v[80:83]\n"
    "v_exp_f32 v138, v138\n"
    "v_mfma_f32_16x16x32_bf16 v[84:87], a[200:203], v[200:203], v[84:87]\n"
    "ds_read_b128 a[136:139], v1 offset:61440\n"
    "v_cvt_pk_bf16_f32 v180, v136, v137\n"
    "v_mfma_f32_16x16x32_bf16 v[84:87], a[204:207], v[204:207], v[84:87]\n"
    "v_exp_f32 v139, v139\n"
    "v_mfma_f32_16x16x32_bf16 v[88:91], a[208:211], v[168:171], v[88:91]\n"
    "s_waitcnt lgkmcnt(4)\n"
    "ds_read_b128 a[140:143], v2 offset:61440\n"
    "v_cvt_pk_bf16_f32 v181, v138, v139\n"
    "v_mfma_f32_16x16x32_bf16 v[88:91], a[212:215], v[172:175], v[88:91]\n"
    "v_exp_f32 v152, v152\n"
    "v_mfma_f32_16x16x32_bf16 v[92:95], a[208:211], v[184:187], v[92:95]\n"
    "ds_read_b128 a[144:147], v1 offset:55296\n"
    "v_mfma_f32_16x16x32_bf16 v[92:95], a[212:215], v[188:191], v[92:95]\n"
    "v_exp_f32 v153, v153\n"
    "v_mfma_f32_16x16x32_bf16 v[96:99], a[208:211], v[200:203], v[96:99]\n"
    "v_cvt_pk_bf16_f32 v196, v152, v153\n"
    "v_mfma_f32_16x16x32_bf16 v[96:99], a[212:215], v[204:207], v[96:99]\n"
    "v_exp_f32 v154, v154\n"
    "v_mfma_f32_16x16x32_bf16 v[100:103], a[216:219], v[168:171], v[100:103]\n"
    "s_waitcnt lgkmcnt(2)\n"
    "v_mfma_f32_16x16x32_bf16 v[100:103], a[220:223], v[172:175], v[100:103]\n"
    "v_exp_f32 v155, v155\n"
    "v_mfma_f32_16x16x32_bf16 v[104:107], a[216:219], v[184:187], v[104:107]\n"
    "v_cvt_pk_bf16_f32 v197, v154, v155\n"
    "v_mfma_f32_16x16x32_bf16 v[104:107], a[220:223], v[188:191], v[104:107]\n"
    "v_exp_f32 v124, v124\n"
    "v_mfma_f32_16x16x32_bf16 v[108:111], a[216:219], v[200:203], v[108:111]\n"
    "s_waitcnt vmcnt(10)\n"
    "s_waitcnt vmcnt(15) lgkmcnt(0)\n"
    "ds_read_b128 v[228:231], v8 offset:8192\n"
    "v_mfma_f32_16x16x32_bf16 v[108:111], a[220:223], v[204:207], v[108:111]\n"
    "s_barrier\n"
    "ds_read_b128 v[220:223], v9 offset:8192\n"
    "v_mfma_f32_16x16x32_bf16 v[112:115], a[96:99], a[0:3], v[208:211]\n"
    "v_exp_f32 v125, v125\n"
    "v_mfma_f32_16x16x32_bf16 v[112:115], a[100:103], a[4:7], v[112:115]\n"
    "s_add_u32 s62, s59, 0\n"
    "v_cvt_pk_bf16_f32 v166, v124, v125\n"
    "v_mfma_f32_16x16x32_bf16 v[112:115], a[104:107], a[8:11], v[112:115]\n"
    "s_add_u32 m0, s60, 32768\n"
    "v_exp_f32 v126, v126\n"
    "v_mfma_f32_16x16x32_bf16 v[112:115], a[108:111], a[12:15], v[112:115]\n"
    "buffer_load_dwordx4 v7, s[44:47], s62 offen nt lds\n"
    "v_mfma_f32_16x16x32_bf16 v[128:131], a[96:99], a[16:19], v[212:215]\n"
    "v_exp_f32 v127, v127\n"
    "v_mfma_f32_16x16x32_bf16 v[128:131], a[100:103], a[20:23], v[128:131]\n"
    "s_waitcnt lgkmcnt(1)\n"
    "v_cvt_pk_bf16_f32 v167, v126, v127\n"
    "v_lshlrev_b32 v246, 16, v228\n"
    "v_and_b32 v228, 0xffff0000, v228\n"
    "v_mul_f32 v246, s56, v246\n"
    "v_mfma_f32_16x16x32_bf16 v[128:131], a[104:107], a[24:27], v[128:131]\n"
    "v_exp_f32 v140, v140\n"
    "v_mul_f32 v228, s56, v228\n"
    "v_cvt_pk_bf16_f32 v228, v246, v228\n"
    "v_accvgpr_write_b32 a80, v228\n"
    "v_lshlrev_b32 v246, 16, v229\n"
    "v_and_b32 v229, 0xffff0000, v229\n"
    "v_mul_f32 v246, s56, v246\n"
    "v_mul_f32 v229, s56, v229\n"
    "v_cvt_pk_bf16_f32 v229, v246, v229\n"
    "v_accvgpr_write_b32 a81, v229\n"
    "v_lshlrev_b32 v246, 16, v230\n"
    "v_and_b32 v230, 0xffff0000, v230\n"
    "v_mul_f32 v246, s56, v246\n"
    "v_mul_f32 v230, s56, v230\n"
    "v_cvt_pk_bf16_f32 v230, v246, v230\n"
    "v_accvgpr_write_b32 a82, v230\n"
    "v_lshlrev_b32 v246, 16, v231\n"
    "v_and_b32 v231, 0xffff0000, v231\n"
    "v_mul_f32 v246, s56, v246\n"
    "v_mul_f32 v231, s56, v231\n"
    "v_cvt_pk_bf16_f32 v231, v246, v231\n"
    "v_accvgpr_write_b32 a83, v231\n"
    "s_waitcnt lgkmcnt(0)\n"
    "v_lshlrev_b32 v246, 16, v220\n"
    "v_and_b32 v220, 0xffff0000, v220\n"
    "v_mul_f32 v246, s56, v246\n"
    "v_mul_f32 v220, s56, v220\n"
    "v_cvt_pk_bf16_f32 v220, v246, v220\n"
    "v_accvgpr_write_b32 a84, v220\n"
    "v_lshlrev_b32 v246, 16, v221\n"
    "v_and_b32 v221, 0xffff0000, v221\n"
    "v_mul_f32 v246, s56, v246\n"
    "v_mul_f32 v221, s56, v221\n"
    "v_cvt_pk_bf16_f32 v221, v246, v221\n"
    "v_accvgpr_write_b32 a85, v221\n"
    "v_lshlrev_b32 v246, 16, v222\n"
    "v_and_b32 v222, 0xffff0000, v222\n"
    "v_mul_f32 v246, s56, v246\n"
    "v_mul_f32 v222, s56, v222\n"
    "v_cvt_pk_bf16_f32 v222, v246, v222\n"
    "v_accvgpr_write_b32 a86, v222\n"
    "v_lshlrev_b32 v246, 16, v223\n"
    "v_and_b32 v223, 0xffff0000, v223\n"
    "v_mul_f32 v246, s56, v246\n"
    "v_mul_f32 v223, s56, v223\n"
    "v_cvt_pk_bf16_f32 v223, v246, v223\n"
    "v_accvgpr_write_b32 a87, v223\n"
    "v_mfma_f32_16x16x32_bf16 v[128:131], a[108:111], a[28:31], v[128:131]\n"
    "v_mfma_f32_16x16x32_bf16 v[144:147], a[96:99], a[32:35], v[216:219]\n"
    "v_exp_f32 v141, v141\n"
    "v_mfma_f32_16x16x32_bf16 v[144:147], a[100:103], a[36:39], v[144:147]\n"
    "v_cvt_pk_bf16_f32 v182, v140, v141\n"
    "v_mfma_f32_16x16x32_bf16 v[144:147], a[104:107], a[40:43], v[144:147]\n"
    "v_exp_f32 v142, v142\n"
    "v_mfma_f32_16x16x32_bf16 v[144:147], a[108:111], a[44:47], v[144:147]\n"
    "v_mfma_f32_16x16x32_bf16 v[116:119], a[112:115], a[0:3], v[208:211]\n"
    "s_add_u32 s63, s59, 128\n"
    "v_exp_f32 v143, v143\n"
    "v_mfma_f32_16x16x32_bf16 v[116:119], a[116:119], a[4:7], v[116:119]\n"
    "s_add_u32 m0, s60, 33792\n"
    "v_cvt_pk_bf16_f32 v183, v142, v143\n"
    "v_mfma_f32_16x16x32_bf16 v[116:119], a[120:123], a[8:11], v[116:119]\n"
    "s_waitcnt vmcnt(9)\n"
    "ds_read_b128 v[224:227], v8 offset:10240\n"
    "v_exp_f32 v156, v156\n"
    "v_mfma_f32_16x16x32_bf16 v[116:119], a[124:127], a[12:15], v[116:119]\n"
    "buffer_load_dwordx4 v7, s[44:47], s63 offen nt lds\n"
    "v_mfma_f32_16x16x32_bf16 v[132:135], a[112:115], a[16:19], v[212:215]\n"
    "v_exp_f32 v157, v157\n"
    "v_mfma_f32_16x16x32_bf16 v[132:135], a[116:119], a[20:23], v[132:135]\n"
    "v_cvt_pk_bf16_f32 v198, v156, v157\n"
    "v_mfma_f32_16x16x32_bf16 v[132:135], a[120:123], a[24:27], v[132:135]\n"
    "v_exp_f32 v158, v158\n"
    "v_mfma_f32_16x16x32_bf16 v[132:135], a[124:127], a[28:31], v[132:135]\n"
    "s_waitcnt lgkmcnt(0)\n"
    "v_lshlrev_b32 v246, 16, v224\n"
    "v_and_b32 v224, 0xffff0000, v224\n"
    "v_mfma_f32_16x16x32_bf16 v[148:151], a[112:115], a[32:35], v[216:219]\n"
    "v_exp_f32 v159, v159\n"
    "v_mfma_f32_16x16x32_bf16 v[148:151], a[116:119], a[36:39], v[148:151]\n"
    "v_cvt_pk_bf16_f32 v199, v158, v159\n"
    "v_mul_f32 v246, s56, v246\n"
    "v_mfma_f32_16x16x32_bf16 v[148:151], a[120:123], a[40:43], v[148:151]\n"
    "v_exp_f32 v112, v112\n"
    "v_mfma_f32_16x16x32_bf16 v[148:151], a[124:127], a[44:47], v[148:151]\n"
    "ds_read_b128 v[228:231], v9 offset:10240\n"
    "v_mul_f32 v224, s56, v224\n"
    "v_cvt_pk_bf16_f32 v224, v246, v224\n"
    "v_accvgpr_write_b32 a88, v224\n"
    "v_lshlrev_b32 v246, 16, v225\n"
    "v_and_b32 v225, 0xffff0000, v225\n"
    "v_mul_f32 v246, s56, v246\n"
    "v_mul_f32 v225, s56, v225\n"
    "v_cvt_pk_bf16_f32 v225, v246, v225\n"
    "v_accvgpr_write_b32 a89, v225\n"
    "v_lshlrev_b32 v246, 16, v226\n"
    "v_and_b32 v226, 0xffff0000, v226\n"
    "v_mul_f32 v246, s56, v246\n"
    "v_mul_f32 v226, s56, v226\n"
    "v_cvt_pk_bf16_f32 v226, v246, v226\n"
    "v_accvgpr_write_b32 a90, v226\n"
    "v_lshlrev_b32 v246, 16, v227\n"
    "v_and_b32 v227, 0xffff0000, v227\n"
    "v_mul_f32 v246, s56, v246\n"
    "v_mul_f32 v227, s56, v227\n"
    "v_cvt_pk_bf16_f32 v227, v246, v227\n"
    "v_accvgpr_write_b32 a91, v227\n"
    "v_mfma_f32_16x16x32_bf16 v[120:123], a[128:131], a[0:3], v[208:211]\n"
    "v_mfma_f32_16x16x32_bf16 v[120:123], a[132:135], a[4:7], v[120:123]\n"
    "v_exp_f32 v113, v113\n"
    "v_mfma_f32_16x16x32_bf16 v[120:123], a[136:139], a[8:11], v[120:123]\n"
    "s_add_u32 s64, s59, 2048\n"
    "v_cvt_pk_bf16_f32 v168, v112, v113\n"
    "v_mfma_f32_16x16x32_bf16 v[120:123], a[140:143], a[12:15], v[120:123]\n"
    "s_add_u32 m0, s60, 34816\n"
    "v_exp_f32 v114, v114\n"
    "v_mfma_f32_16x16x32_bf16 v[136:139], a[128:131], a[16:19], v[212:215]\n"
    "buffer_load_dwordx4 v7, s[44:47], s64 offen nt lds\n"
    "v_mfma_f32_16x16x32_bf16 v[136:139], a[132:135], a[20:23], v[136:139]\n"
    "v_exp_f32 v115, v115\n"
    "v_mfma_f32_16x16x32_bf16 v[136:139], a[136:139], a[24:27], v[136:139]\n"
    "s_waitcnt lgkmcnt(0)\n"
    "v_cvt_pk_bf16_f32 v169, v114, v115\n"
    "v_lshlrev_b32 v246, 16, v228\n"
    "v_mfma_f32_16x16x32_bf16 v[136:139], a[140:143], a[28:31], v[136:139]\n"
    "v_and_b32 v228, 0xffff0000, v228\n"
    "v_mul_f32 v246, s56, v246\n"
    "v_mfma_f32_16x16x32_bf16 v[152:155], a[128:131], a[32:35], v[216:219]\n"
    "v_exp_f32 v128, v128\n"
    "v_mfma_f32_16x16x32_bf16 v[152:155], a[132:135], a[36:39], v[152:155]\n"
    "v_mul_f32 v228, s56, v228\n"
    "v_cvt_pk_bf16_f32 v228, v246, v228\n"
    "v_mfma_f32_16x16x32_bf16 v[152:155], a[136:139], a[40:43], v[152:155]\n"
    "v_exp_f32 v129, v129\n"
    "v_mfma_f32_16x16x32_bf16 v[152:155], a[140:143], a[44:47], v[152:155]\n"
    "ds_read_b64_tr_b16 a[160:161], v3 offset:0\n"
    "ds_read_b64_tr_b16 a[162:163], v3 offset:4096\n"
    "v_mfma_f32_16x16x32_bf16 v[124:127], a[144:147], a[0:3], v[208:211]\n"
    "v_exp_f32 v130, v130\n"
    "v_mfma_f32_16x16x32_bf16 v[124:127], a[148:151], a[4:7], v[124:127]\n"
    "ds_read_b64_tr_b16 a[164:165], v3 offset:8192\n"
    "v_cvt_pk_bf16_f32 v184, v128, v129\n"
    "v_mfma_f32_16x16x32_bf16 v[124:127], a[152:155], a[8:11], v[124:127]\n"
    "ds_read_b64_tr_b16 a[166:167], v3 offset:12288\n"
    "ds_read_b64_tr_b16 a[170:171], v4 offset:4096\n"
    "v_mfma_f32_16x16x32_bf16 v[124:127], a[156:159], a[12:15], v[124:127]\n"
    "v_exp_f32 v131, v131\n"
    "v_mfma_f32_16x16x32_bf16 v[140:143], a[144:147], a[16:19], v[212:215]\n"
    "s_add_u32 s65, s59, 2176\n"
    "ds_read_b64_tr_b16 a[168:169], v4 offset:0\n"
    "v_cvt_pk_bf16_f32 v185, v130, v131\n"
    "v_mfma_f32_16x16x32_bf16 v[140:143], a[148:151], a[20:23], v[140:143]\n"
    "s_add_u32 m0, s60, 35840\n"
    "v_exp_f32 v144, v144\n"
    "v_mfma_f32_16x16x32_bf16 v[140:143], a[152:155], a[24:27], v[140:143]\n"
    "buffer_load_dwordx4 v7, s[44:47], s65 offen nt lds\n"
    "v_mfma_f32_16x16x32_bf16 v[140:143], a[156:159], a[28:31], v[140:143]\n"
    "v_exp_f32 v145, v145\n"
    "v_mfma_f32_16x16x32_bf16 v[156:159], a[144:147], a[32:35], v[216:219]\n"
    "ds_read_b64_tr_b16 a[172:173], v4 offset:8192\n"
    "ds_read_b64_tr_b16 a[174:175], v4 offset:12288\n"
    "v_mfma_f32_16x16x32_bf16 v[156:159], a[148:151], a[36:39], v[156:159]\n"
    "v_cvt_pk_bf16_f32 v200, v144, v145\n"
    "v_accvgpr_write_b32 a92, v228\n"
    "v_lshlrev_b32 v246, 16, v229\n"
    "v_and_b32 v229, 0xffff0000, v229\n"
    "v_mfma_f32_16x16x32_bf16 v[156:159], a[152:155], a[40:43], v[156:159]\n"
    "v_exp_f32 v146, v146\n"
    "v_mul_f32 v246, s56, v246\n"
    "v_mul_f32 v229, s56, v229\n"
    "v_mfma_f32_16x16x32_bf16 v[156:159], a[156:159], a[44:47], v[156:159]\n"
    "ds_read_b64_tr_b16 a[176:177], v3 offset:512\n"
    "ds_read_b64_tr_b16 a[178:179], v3 offset:4608\n"
    "v_cvt_pk_bf16_f32 v229, v246, v229\n"
    "v_accvgpr_write_b32 a93, v229\n"
    "v_mfma_f32_16x16x32_bf16 v[232:235], a[144:147], a[48:51], 0\n"
    "v_exp_f32 v147, v147\n"
    "v_lshlrev_b32 v246, 16, v230\n"
    "v_and_b32 v230, 0xffff0000, v230\n"
    "v_mfma_f32_16x16x32_bf16 v[232:235], a[148:151], a[52:55], v[232:235]\n"
    "ds_read_b64_tr_b16 a[180:181], v3 offset:8704\n"
    "ds_read_b64_tr_b16 a[182:183], v3 offset:12800\n"
    "v_mul_f32 v246, s56, v246\n"
    "v_mul_f32 v230, s56, v230\n"
    "v_mfma_f32_16x16x32_bf16 v[232:235], a[152:155], a[56:59], v[232:235]\n"
    "v_exp_f32 v116, v116\n"
    "v_cvt_pk_bf16_f32 v230, v246, v230\n"
    "v_accvgpr_write_b32 a94, v230\n"
    "v_mfma_f32_16x16x32_bf16 v[232:235], a[156:159], a[60:63], v[232:235]\n"
    "ds_read_b64_tr_b16 a[190:191], v4 offset:12800\n"
    "v_cvt_pk_bf16_f32 v201, v146, v147\n"
    "v_lshlrev_b32 v246, 16, v231\n"
    "v_and_b32 v231, 0xffff0000, v231\n"
    "v_mfma_f32_16x16x32_bf16 v[236:239], a[144:147], a[64:67], 0\n"
    "ds_read_b64_tr_b16 a[184:185], v4 offset:512\n"
    "ds_read_b64_tr_b16 a[186:187], v4 offset:4608\n"
    "v_mul_f32 v246, s56, v246\n"
    "v_mul_f32 v231, s56, v231\n"
    "v_cvt_pk_bf16_f32 v231, v246, v231\n"
    "v_accvgpr_write_b32 a95, v231\n"
    "v_mfma_f32_16x16x32_bf16 v[236:239], a[148:151], a[68:71], v[236:239]\n"
    "v_exp_f32 v117, v117\n"
    "v_mfma_f32_16x16x32_bf16 v[236:239], a[152:155], a[72:75], v[236:239]\n"
    "s_add_u32 s62, s57, 49152\n"
    "ds_read_b64_tr_b16 a[188:189], v4 offset:8704\n"
    "v_cvt_pk_bf16_f32 v170, v116, v117\n"
    "v_mfma_f32_16x16x32_bf16 v[236:239], a[156:159], a[76:79], v[236:239]\n"
    "s_add_u32 m0, s58, 49152\n"
    "v_exp_f32 v118, v118\n"
    "v_max3_f32 v244, v232, v233, v234\n"
    "v_mfma_f32_16x16x32_bf16 v[240:243], a[144:147], a[80:83], 0\n"
    "buffer_load_dwordx4 v5, s[40:43], s62 offen nt lds\n"
    "v_max_f32 v244, v244, v235\n"
    "v_mov_b32 v245, v244\n"
    "v_mfma_f32_16x16x32_bf16 v[240:243], a[148:151], a[84:87], v[240:243]\n"
    "v_exp_f32 v119, v119\n"
    "v_permlane16_swap_b32 v244, v245\n"
    "v_mfma_f32_16x16x32_bf16 v[240:243], a[152:155], a[88:91], v[240:243]\n"
    "ds_read_b64_tr_b16 a[196:197], v3 offset:9216\n"
    "v_cvt_pk_bf16_f32 v171, v118, v119\n"
    "v_mfma_f32_16x16x32_bf16 v[240:243], a[156:159], a[92:95], v[240:243]\n"
    "ds_read_b64_tr_b16 a[192:193], v3 offset:1024\n"
    "ds_read_b64_tr_b16 a[194:195], v3 offset:5120\n"
    "v_max3_f32 v250, v236, v237, v238\n"
    "v_mfma_f32_16x16x32_bf16 a[224:227], a[236:239], v[160:163], a[224:227]\n"
    "v_exp_f32 v132, v132\n"
    "v_max_f32 v250, v250, v239\n"
    "v_mov_b32 v251, v250\n"
    "v_mfma_f32_16x16x32_bf16 a[224:227], a[236:239], v[164:167], a[224:227]\n"
    "ds_read_b128 a[96:99], v1 offset:0\n"
    "v_max_f32 v244, v244, v245\n"
    "v_mov_b32 v245, v244\n"
    "v_permlane16_swap_b32 v250, v251\n"
    "v_mfma_f32_16x16x32_bf16 a[228:231], a[236:239], v[176:179], a[228:231]\n"
    "v_exp_f32 v133, v133\n"
    "v_max_f32 v250, v250, v251\n"
    "v_mov_b32 v251, v250\n"
    "v_mfma_f32_16x16x32_bf16 a[228:231], a[236:239], v[180:183], a[228:231]\n"
    "ds_read_b64_tr_b16 a[198:199], v3 offset:13312\n"
    "v_cvt_pk_bf16_f32 v186, v132, v133\n"
    "v_permlane32_swap_b32 v250, v251\n"
    "v_mfma_f32_16x16x32_bf16 a[232:235], a[236:239], v[192:195], a[232:235]\n"
    "v_exp_f32 v134, v134\n"
    "v_max_f32 v250, v250, v251\n"
    "v_sub_f32 v212, 0, v250\n"
    "v_mfma_f32_16x16x32_bf16 a[232:235], a[236:239], v[196:199], a[232:235]\n"
    "s_waitcnt lgkmcnt(4)\n"
    "ds_read_b128 a[100:103], v2 offset:0\n"
    "v_permlane32_swap_b32 v244, v245\n"
    "v_max_f32 v244, v244, v245\n"
    "v_mov_b32 v213, v212\n"
    "v_mfma_f32_16x16x32_bf16 v[16:19], a[160:163], v[160:163], v[16:19]\n"
    "s_add_u32 s63, s57, 51200\n"
    "ds_read_b64_tr_b16 a[200:201], v4 offset:1024\n"
    "ds_read_b64_tr_b16 a[202:203], v4 offset:5120\n"
    "v_sub_f32 v208, 0, v244\n"
    "v_mov_b32 v209, v208\n"
    "v_mfma_f32_16x16x32_bf16 v[16:19], a[164:167], v[164:167], v[16:19]\n"
    "s_add_u32 m0, s58, 50176\n"
    "v_exp_f32 v135, v135\n"
    "v_mov_b32 v210, v208\n"
    "v_mov_b32 v211, v208\n"
    "v_mfma_f32_16x16x32_bf16 v[20:23], a[160:163], v[176:179], v[20:23]\n"
    "buffer_load_dwordx4 v6, s[40:43], s63 offen nt lds\n"
    "v_max3_f32 v244, v240, v241, v242\n"
    "v_max_f32 v244, v244, v243\n"
    "v_mfma_f32_16x16x32_bf16 v[20:23], a[164:167], v[180:183], v[20:23]\n"
    "v_exp_f32 v148, v148\n"
    "v_mov_b32 v245, v244\n"
    "v_mov_b32 v214, v212\n"
    "v_mfma_f32_16x16x32_bf16 v[24:27], a[160:163], v[192:195], v[24:27]\n"
    "ds_read_b128 a[104:107], v1 offset:8192\n"
    "v_cvt_pk_bf16_f32 v187, v134, v135\n"
    "v_permlane16_swap_b32 v244, v245\n"
    "v_mfma_f32_16x16x32_bf16 v[24:27], a[164:167], v[196:199], v[24:27]\n"
    "v_exp_f32 v149, v149\n"
    "v_max_f32 v244, v244, v245\n"
    "v_mov_b32 v245, v244\n"
    "v_mfma_f32_16x16x32_bf16 v[28:31], a[168:171], v[160:163], v[28:31]\n"
    "ds_read_b128 a[108:111], v2 offset:8192\n"
    "v_cvt_pk_bf16_f32 v202, v148, v149\n"
    "v_permlane32_swap_b32 v244, v245\n"
    "v_mfma_f32_16x16x32_bf16 v[28:31], a[172:175], v[164:167], v[28:31]\n"
    "ds_read_b64_tr_b16 a[204:205], v4 offset:9216\n"
    "ds_read_b64_tr_b16 a[206:207], v4 offset:13312\n"
    "v_max_f32 v244, v244, v245\n"
    "v_sub_f32 v216, 0, v244\n"
    "v_mfma_f32_16x16x32_bf16 v[32:35], a[168:171], v[176:179], v[32:35]\n"
    "v_exp_f32 v150, v150\n"
    "v_mov_b32 v217, v216\n"
    "v_mov_b32 v218, v216\n"
    "v_mfma_f32_16x16x32_bf16 v[32:35], a[172:175], v[180:183], v[32:35]\n"
    "ds_read_b128 a[112:115], v1 offset:2048\n"
    "v_mov_b32 v215, v212\n"
    "v_mov_b32 v219, v216\n"
    "v_mfma_f32_16x16x32_bf16 v[36:39], a[168:171], v[192:195], v[36:39]\n"
    "v_exp_f32 v151, v151\n"
    "v_mfma_f32_16x16x32_bf16 v[36:39], a[172:175], v[196:199], v[36:39]\n"
    "ds_read_b128 a[116:119], v2 offset:2048\n"
    "v_cvt_pk_bf16_f32 v203, v150, v151\n"
    "v_mfma_f32_16x16x32_bf16 v[40:43], a[176:179], v[160:163], v[40:43]\n"
    "v_exp_f32 v120, v120\n"
    "v_mfma_f32_16x16x32_bf16 v[40:43], a[180:183], v[164:167], v[40:43]\n"
    "ds_read_b64_tr_b16 a[208:209], v3 offset:1536\n"
    "ds_read_b64_tr_b16 a[210:211], v3 offset:5632\n"
    "v_mfma_f32_16x16x32_bf16 v[44:47], a[176:179], v[176:179], v[44:47]\n"
    "s_add_u32 s64, s57, 53248\n"
    "ds_read_b64_tr_b16 a[212:213], v3 offset:9728\n"
    "ds_read_b64_tr_b16 a[214:215], v3 offset:13824\n"
    "v_mfma_f32_16x16x32_bf16 v[44:47], a[180:183], v[180:183], v[44:47]\n"
    "s_add_u32 m0, s58, 51200\n"
    "v_exp_f32 v121, v121\n"
    "v_mfma_f32_16x16x32_bf16 v[48:51], a[176:179], v[192:195], v[48:51]\n"
    "buffer_load_dwordx4 v5, s[40:43], s64 offen nt lds\n"
    "v_mfma_f32_16x16x32_bf16 v[48:51], a[180:183], v[196:199], v[48:51]\n"
    "v_exp_f32 v122, v122\n"
    "v_mfma_f32_16x16x32_bf16 v[52:55], a[184:187], v[160:163], v[52:55]\n"
    "ds_read_b128 a[120:123], v1 offset:10240\n"
    "v_cvt_pk_bf16_f32 v172, v120, v121\n"
    "v_mfma_f32_16x16x32_bf16 v[52:55], a[188:191], v[164:167], v[52:55]\n"
    "ds_read_b64_tr_b16 a[216:217], v4 offset:1536\n"
    "ds_read_b64_tr_b16 a[218:219], v4 offset:5632\n"
    "v_mfma_f32_16x16x32_bf16 v[56:59], a[184:187], v[176:179], v[56:59]\n"
    "v_exp_f32 v123, v123\n"
    "v_mfma_f32_16x16x32_bf16 v[56:59], a[188:191], v[180:183], v[56:59]\n"
    "ds_read_b128 a[124:127], v2 offset:10240\n"
    "v_cvt_pk_bf16_f32 v173, v122, v123\n"
    "v_mfma_f32_16x16x32_bf16 v[60:63], a[184:187], v[192:195], v[60:63]\n"
    "v_exp_f32 v136, v136\n"
    "v_mfma_f32_16x16x32_bf16 v[60:63], a[188:191], v[196:199], v[60:63]\n"
    "s_waitcnt lgkmcnt(4)\n"
    "ds_read_b128 a[128:131], v1 offset:4096\n"
    "v_mfma_f32_16x16x32_bf16 v[64:67], a[192:195], v[160:163], v[64:67]\n"
    "v_exp_f32 v137, v137\n"
    "v_mfma_f32_16x16x32_bf16 v[64:67], a[196:199], v[164:167], v[64:67]\n"
    "ds_read_b128 a[132:135], v2 offset:4096\n"
    "v_cvt_pk_bf16_f32 v188, v136, v137\n"
    "v_mfma_f32_16x16x32_bf16 v[68:71], a[192:195], v[176:179], v[68:71]\n"
    "s_add_u32 s65, s57, 55296\n"
    "ds_read_b64_tr_b16 a[220:221], v4 offset:9728\n"
    "ds_read_b64_tr_b16 a[222:223], v4 offset:13824\n"
    "v_mfma_f32_16x16x32_bf16 v[68:71], a[196:199], v[180:183], v[68:71]\n"
    "s_add_u32 m0, s58, 52224\n"
    "v_exp_f32 v138, v138\n"
    "v_mfma_f32_16x16x32_bf16 v[72:75], a[192:195], v[192:195], v[72:75]\n"
    "buffer_load_dwordx4 v6, s[40:43], s65 offen nt lds\n"
    "ds_read_b128 a[156:159], v2 offset:14336\n"
    "v_mfma_f32_16x16x32_bf16 v[72:75], a[196:199], v[196:199], v[72:75]\n"
    "v_exp_f32 v139, v139\n"
    "v_mfma_f32_16x16x32_bf16 v[76:79], a[200:203], v[160:163], v[76:79]\n"
    "ds_read_b128 a[136:139], v1 offset:12288\n"
    "v_cvt_pk_bf16_f32 v189, v138, v139\n"
    "v_mfma_f32_16x16x32_bf16 v[76:79], a[204:207], v[164:167], v[76:79]\n"
    "v_exp_f32 v152, v152\n"
    "v_mfma_f32_16x16x32_bf16 v[80:83], a[200:203], v[176:179], v[80:83]\n"
    "ds_read_b128 a[140:143], v2 offset:12288\n"
    "v_mfma_f32_16x16x32_bf16 v[80:83], a[204:207], v[180:183], v[80:83]\n"
    "ds_read_b128 a[152:155], v1 offset:14336\n"
    "v_mfma_f32_16x16x32_bf16 v[84:87], a[200:203], v[192:195], v[84:87]\n"
    "v_exp_f32 v153, v153\n"
    "v_mfma_f32_16x16x32_bf16 v[84:87], a[204:207], v[196:199], v[84:87]\n"
    "ds_read_b128 a[144:147], v1 offset:6144\n"
    "v_cvt_pk_bf16_f32 v204, v152, v153\n"
    "v_mfma_f32_16x16x32_bf16 v[88:91], a[208:211], v[160:163], v[88:91]\n"
    "v_exp_f32 v154, v154\n"
    "v_mfma_f32_16x16x32_bf16 v[88:91], a[212:215], v[164:167], v[88:91]\n"
    "ds_read_b128 a[148:151], v2 offset:6144\n"
    "v_mfma_f32_16x16x32_bf16 v[92:95], a[208:211], v[176:179], v[92:95]\n"
    "v_exp_f32 v155, v155\n"
    "v_mfma_f32_16x16x32_bf16 v[92:95], a[212:215], v[180:183], v[92:95]\n"
    "v_cvt_pk_bf16_f32 v205, v154, v155\n"
    "v_mfma_f32_16x16x32_bf16 v[96:99], a[208:211], v[192:195], v[96:99]\n"
    "v_mfma_f32_16x16x32_bf16 v[96:99], a[212:215], v[196:199], v[96:99]\n"
    "s_waitcnt lgkmcnt(2)\n"
    "v_exp_f32 v124, v124\n"
    "v_mfma_f32_16x16x32_bf16 v[100:103], a[216:219], v[160:163], v[100:103]\n"
    "v_mfma_f32_16x16x32_bf16 v[100:103], a[220:223], v[164:167], v[100:103]\n"
    "v_exp_f32 v125, v125\n"
    "v_mfma_f32_16x16x32_bf16 v[104:107], a[216:219], v[176:179], v[104:107]\n"
    "v_cvt_pk_bf16_f32 v174, v124, v125\n"
    "v_mfma_f32_16x16x32_bf16 v[104:107], a[220:223], v[180:183], v[104:107]\n"
    "v_exp_f32 v126, v126\n"
    "v_mfma_f32_16x16x32_bf16 v[108:111], a[216:219], v[192:195], v[108:111]\n"
    "s_waitcnt vmcnt(12) lgkmcnt(0)\n"
    "v_mfma_f32_16x16x32_bf16 v[108:111], a[220:223], v[196:199], v[108:111]\n"
    "s_barrier\n"
    "v_mfma_f32_16x16x32_bf16 v[112:115], a[96:99], a[48:51], v[208:211]\n"
    "v_exp_f32 v127, v127\n"
    "v_mfma_f32_16x16x32_bf16 v[112:115], a[100:103], a[52:55], v[112:115]\n"
    "s_add_u32 s62, s59, 16384\n"
    "v_cvt_pk_bf16_f32 v175, v126, v127\n"
    "v_mfma_f32_16x16x32_bf16 v[112:115], a[104:107], a[56:59], v[112:115]\n"
    "s_add_u32 m0, s60, 0\n"
    "v_exp_f32 v140, v140\n"
    "v_mfma_f32_16x16x32_bf16 v[112:115], a[108:111], a[60:63], v[112:115]\n"
    "buffer_load_dwordx4 v7, s[44:47], s62 offen nt lds\n"
    "v_mfma_f32_16x16x32_bf16 v[128:131], a[96:99], a[64:67], v[212:215]\n"
    "s_add_u32 s63, s59, 16512\n"
    "v_exp_f32 v141, v141\n"
    "v_mfma_f32_16x16x32_bf16 v[128:131], a[100:103], a[68:71], v[128:131]\n"
    "s_add_u32 m0, s60, 1024\n"
    "v_cvt_pk_bf16_f32 v190, v140, v141\n"
    "v_mfma_f32_16x16x32_bf16 v[128:131], a[104:107], a[72:75], v[128:131]\n"
    "s_add_u32 s64, s59, 18432\n"
    "buffer_load_dwordx4 v7, s[44:47], s63 offen nt lds\n"
    "v_mfma_f32_16x16x32_bf16 v[128:131], a[108:111], a[76:79], v[128:131]\n"
    "s_add_u32 m0, s60, 2048\n"
    "v_exp_f32 v142, v142\n"
    "v_mfma_f32_16x16x32_bf16 v[144:147], a[96:99], a[80:83], v[216:219]\n"
    "buffer_load_dwordx4 v7, s[44:47], s64 offen nt lds\n"
    "v_mfma_f32_16x16x32_bf16 v[144:147], a[100:103], a[84:87], v[144:147]\n"
    "v_exp_f32 v143, v143\n"
    "v_mfma_f32_16x16x32_bf16 v[144:147], a[104:107], a[88:91], v[144:147]\n"
    "s_add_u32 s65, s59, 18560\n"
    "v_cvt_pk_bf16_f32 v191, v142, v143\n"
    "v_mfma_f32_16x16x32_bf16 v[144:147], a[108:111], a[92:95], v[144:147]\n"
    "s_add_u32 m0, s60, 3072\n"
    "v_exp_f32 v156, v156\n"
    "v_mfma_f32_16x16x32_bf16 v[116:119], a[112:115], a[48:51], v[208:211]\n"
    "Lpg4%=:\n"
    "s_cbranch_scc1 Lpg8%=\n"
    "buffer_load_dwordx4 v7, s[44:47], s65 offen nt lds\n"
    "v_mfma_f32_16x16x32_bf16 v[116:119], a[116:119], a[52:55], v[116:119]\n"
    "s_add_u32 s62, s57, 65536\n"
    "v_mfma_f32_16x16x32_bf16 v[116:119], a[120:123], a[56:59], v[116:119]\n"
    "s_add_u32 m0, s58, 0\n"
    "v_exp_f32 v157, v157\n"
    "v_mfma_f32_16x16x32_bf16 v[116:119], a[124:127], a[60:63], v[116:119]\n"
    "s_add_u32 s63, s57, 67584\n"
    "buffer_load_dwordx4 v5, s[40:43], s62 offen nt lds\n"
    "v_mfma_f32_16x16x32_bf16 v[132:135], a[112:115], a[64:67], v[212:215]\n"
    "s_add_u32 m0, s58, 1024\n"
    "v_exp_f32 v158, v158\n"
    "v_mfma_f32_16x16x32_bf16 v[132:135], a[116:119], a[68:71], v[132:135]\n"
    "buffer_load_dwordx4 v6, s[40:43], s63 offen nt lds\n"
    "v_mfma_f32_16x16x32_bf16 v[132:135], a[120:123], a[72:75], v[132:135]\n"
    "s_add_u32 s64, s57, 69632\n"
    "v_exp_f32 v159, v159\n"
    "v_mfma_f32_16x16x32_bf16 v[132:135], a[124:127], a[76:79], v[132:135]\n"
    "s_add_u32 m0, s58, 2048\n"
    "v_cvt_pk_bf16_f32 v206, v156, v157\n"
    "v_cvt_pk_bf16_f32 v207, v158, v159\n"
    "v_mfma_f32_16x16x32_bf16 v[148:151], a[112:115], a[80:83], v[216:219]\n"
    "buffer_load_dwordx4 v5, s[40:43], s64 offen nt lds\n"
    "v_mfma_f32_16x16x32_bf16 v[148:151], a[116:119], a[84:87], v[148:151]\n"
    "v_exp_f32 v112, v112\n"
    "v_mfma_f32_16x16x32_bf16 v[148:151], a[120:123], a[88:91], v[148:151]\n"
    "s_add_u32 s65, s57, 71680\n"
    "ds_read_b64_tr_b16 a[160:161], v3 offset:16384\n"
    "ds_read_b64_tr_b16 a[162:163], v3 offset:20480\n"
    "v_mfma_f32_16x16x32_bf16 v[148:151], a[124:127], a[92:95], v[148:151]\n"
    "s_add_u32 m0, s58, 3072\n"
    "v_exp_f32 v113, v113\n"
    "v_mfma_f32_16x16x32_bf16 v[120:123], a[128:131], a[48:51], v[208:211]\n"
    "buffer_load_dwordx4 v6, s[40:43], s65 offen nt lds\n"
    "v_mfma_f32_16x16x32_bf16 v[120:123], a[132:135], a[52:55], v[120:123]\n"
    "v_exp_f32 v114, v114\n"
    "v_mfma_f32_16x16x32_bf16 v[120:123], a[136:139], a[56:59], v[120:123]\n"
    "ds_read_b64_tr_b16 a[164:165], v3 offset:24576\n"
    "v_cvt_pk_bf16_f32 v160, v112, v113\n"
    "v_mfma_f32_16x16x32_bf16 v[120:123], a[140:143], a[60:63], v[120:123]\n"
    "v_exp_f32 v115, v115\n"
    "v_mfma_f32_16x16x32_bf16 v[136:139], a[128:131], a[64:67], v[212:215]\n"
    "ds_read_b64_tr_b16 a[166:167], v3 offset:28672\n"
    "v_cvt_pk_bf16_f32 v161, v114, v115\n"
    "v_mfma_f32_16x16x32_bf16 v[136:139], a[132:135], a[68:71], v[136:139]\n"
    "v_exp_f32 v128, v128\n"
    "v_mfma_f32_16x16x32_bf16 v[136:139], a[136:139], a[72:75], v[136:139]\n"
    "ds_read_b64_tr_b16 a[168:169], v4 offset:16384\n"
    "ds_read_b64_tr_b16 a[170:171], v4 offset:20480\n"
    "v_mfma_f32_16x16x32_bf16 v[136:139], a[140:143], a[76:79], v[136:139]\n"
    "v_exp_f32 v129, v129\n"
    "v_mfma_f32_16x16x32_bf16 v[152:155], a[128:131], a[80:83], v[216:219]\n"
    "ds_read_b64_tr_b16 a[172:173], v4 offset:24576\n"
    "ds_read_b64_tr_b16 a[174:175], v4 offset:28672\n"
    "v_mfma_f32_16x16x32_bf16 v[152:155], a[132:135], a[84:87], v[152:155]\n"
    "v_exp_f32 v130, v130\n"
    "v_mfma_f32_16x16x32_bf16 v[152:155], a[136:139], a[88:91], v[152:155]\n"
    "ds_read_b64_tr_b16 a[176:177], v3 offset:16896\n"
    "v_cvt_pk_bf16_f32 v176, v128, v129\n"
    "v_mfma_f32_16x16x32_bf16 v[152:155], a[140:143], a[92:95], v[152:155]\n"
    "v_exp_f32 v131, v131\n"
    "v_mfma_f32_16x16x32_bf16 v[124:127], a[144:147], a[48:51], v[208:211]\n"
    "ds_read_b64_tr_b16 a[178:179], v3 offset:20992\n"
    "ds_read_b64_tr_b16 a[180:181], v3 offset:25088\n"
    "v_mfma_f32_16x16x32_bf16 v[124:127], a[148:151], a[52:55], v[124:127]\n"
    "v_exp_f32 v144, v144\n"
    "v_mfma_f32_16x16x32_bf16 v[124:127], a[152:155], a[56:59], v[124:127]\n"
    "ds_read_b64_tr_b16 a[182:183], v3 offset:29184\n"
    "v_cvt_pk_bf16_f32 v177, v130, v131\n"
    "v_mfma_f32_16x16x32_bf16 v[124:127], a[156:159], a[60:63], v[124:127]\n"
    "v_exp_f32 v145, v145\n"
    "v_mfma_f32_16x16x32_bf16 v[140:143], a[144:147], a[64:67], v[212:215]\n"
    "ds_read_b64_tr_b16 a[184:185], v4 offset:16896\n"
    "v_cvt_pk_bf16_f32 v192, v144, v145\n"
    "v_mfma_f32_16x16x32_bf16 v[140:143], a[148:151], a[68:71], v[140:143]\n"
    "v_exp_f32 v146, v146\n"
    "v_mfma_f32_16x16x32_bf16 v[140:143], a[152:155], a[72:75], v[140:143]\n"
    "ds_read_b64_tr_b16 a[186:187], v4 offset:20992\n"
    "ds_read_b64_tr_b16 a[188:189], v4 offset:25088\n"
    "v_mfma_f32_16x16x32_bf16 v[140:143], a[156:159], a[76:79], v[140:143]\n"
    "v_exp_f32 v147, v147\n"
    "v_mfma_f32_16x16x32_bf16 v[156:159], a[144:147], a[80:83], v[216:219]\n"
    "ds_read_b64_tr_b16 a[190:191], v4 offset:29184\n"
    "v_cvt_pk_bf16_f32 v193, v146, v147\n"
    "v_mfma_f32_16x16x32_bf16 v[156:159], a[148:151], a[84:87], v[156:159]\n"
    "ds_read_b64_tr_b16 a[196:197], v3 offset:25600\n"
    "ds_read_b64_tr_b16 a[198:199], v3 offset:29696\n"
    "v_mfma_f32_16x16x32_bf16 v[156:159], a[152:155], a[88:91], v[156:159]\n"
    "v_exp_f32 v116, v116\n"
    "v_mfma_f32_16x16x32_bf16 v[156:159], a[156:159], a[92:95], v[156:159]\n"
    "ds_read_b64_tr_b16 a[192:193], v3 offset:17408\n"
    "ds_read_b64_tr_b16 a[194:195], v3 offset:21504\n"
    "v_mfma_f32_16x16x32_bf16 a[224:227], a[236:239], v[168:171], a[224:227]\n"
    "v_exp_f32 v117, v117\n"
    "v_mfma_f32_16x16x32_bf16 a[224:227], a[236:239], v[172:175], a[224:227]\n"
    "ds_read_b128 a[96:99], v1 offset:16384\n"
    "v_cvt_pk_bf16_f32 v162, v116, v117\n"
    "v_mfma_f32_16x16x32_bf16 a[228:231], a[236:239], v[184:187], a[228:231]\n"
    "v_exp_f32 v118, v118\n"
    "v_mfma_f32_16x16x32_bf16 a[228:231], a[236:239], v[188:191], a[228:231]\n"
    "ds_read_b128 a[100:103], v2 offset:16384\n"
    "v_mfma_f32_16x16x32_bf16 a[232:235], a[236:239], v[200:203], a[232:235]\n"
    "v_exp_f32 v119, v119\n"
    "v_mfma_f32_16x16x32_bf16 a[232:235], a[236:239], v[204:207], a[232:235]\n"
    "s_waitcnt lgkmcnt(4)\n"
    "ds_read_b128 a[104:107], v1 offset:24576\n"
    "v_cvt_pk_bf16_f32 v163, v118, v119\n"
    "v_mfma_f32_16x16x32_bf16 v[16:19], a[160:163], v[168:171], v[16:19]\n"
    "v_exp_f32 v132, v132\n"
    "v_mfma_f32_16x16x32_bf16 v[16:19], a[164:167], v[172:175], v[16:19]\n"
    "ds_read_b64_tr_b16 a[200:201], v4 offset:17408\n"
    "ds_read_b64_tr_b16 a[202:203], v4 offset:21504\n"
    "v_accvgpr_read_b32 v248, a228\n"
    "v_cmp_ngt_f32 s[90:91], s70, v248\n"
    "v_mfma_f32_16x16x32_bf16 v[20:23], a[160:163], v[184:187], v[20:23]\n"
    "v_exp_f32 v133, v133\n"
    "v_rcp_f32 v248, v248\n"
    "v_mfma_f32_16x16x32_bf16 v[20:23], a[164:167], v[188:191], v[20:23]\n"
    "ds_read_b128 a[108:111], v2 offset:24576\n"
    "v_cvt_pk_bf16_f32 v178, v132, v133\n"
    "v_accvgpr_read_b32 v249, a232\n"
    "v_cmp_ngt_f32 s[92:93], s70, v249\n"
    "v_mfma_f32_16x16x32_bf16 v[24:27], a[160:163], v[200:203], v[24:27]\n"
    "v_exp_f32 v134, v134\n"
    "v_rcp_f32 v249, v249\n"
    "v_mfma_f32_16x16x32_bf16 v[24:27], a[164:167], v[204:207], v[24:27]\n"
    "ds_read_b64_tr_b16 a[204:205], v4 offset:25600\n"
    "ds_read_b64_tr_b16 a[206:207], v4 offset:29696\n"
    "v_mfma_f32_16x16x32_bf16 v[28:31], a[168:171], v[168:171], v[28:31]\n"
    "v_exp_f32 v135, v135\n"
    "v_mfma_f32_16x16x32_bf16 v[28:31], a[172:175], v[172:175], v[28:31]\n"
    "ds_read_b128 a[112:115], v1 offset:18432\n"
    "v_cvt_pk_bf16_f32 v179, v134, v135\n"
    "v_mul_f32 v22, v248, v22\n"
    "v_mul_f32 v23, v248, v23\n"
    "v_mfma_f32_16x16x32_bf16 v[32:35], a[168:171], v[184:187], v[32:35]\n"
    "v_exp_f32 v148, v148\n"
    "v_mul_f32 v20, v248, v20\n"
    "v_mul_f32 v21, v248, v21\n"
    "v_mfma_f32_16x16x32_bf16 v[32:35], a[172:175], v[188:191], v[32:35]\n"
    "ds_read_b128 a[116:119], v2 offset:18432\n"
    "v_accvgpr_read_b32 v247, a224\n"
    "v_cmp_ngt_f32 s[88:89], s70, v247\n"
    "v_rcp_f32 v247, v247\n"
    "v_mfma_f32_16x16x32_bf16 v[36:39], a[168:171], v[200:203], v[36:39]\n"
    "v_exp_f32 v149, v149\n"
    "v_mul_f32 v16, v247, v16\n"
    "v_mul_f32 v17, v247, v17\n"
    "v_mfma_f32_16x16x32_bf16 v[36:39], a[172:175], v[204:207], v[36:39]\n"
    "ds_read_b64_tr_b16 a[208:209], v3 offset:17920\n"
    "ds_read_b64_tr_b16 a[210:211], v3 offset:22016\n"
    "v_mul_f32 v19, v247, v19\n"
    "v_cvt_pk_bf16_f32 v16, v16, v17\n"
    "v_mfma_f32_16x16x32_bf16 v[40:43], a[176:179], v[168:171], v[40:43]\n"
    "v_exp_f32 v150, v150\n"
    "v_cvt_pk_bf16_f32 v20, v20, v21\n"
    "v_cvt_pk_bf16_f32 v21, v22, v23\n"
    "v_mfma_f32_16x16x32_bf16 v[40:43], a[180:183], v[172:175], v[40:43]\n"
    "ds_read_b128 a[120:123], v1 offset:26624\n"
    "v_cvt_pk_bf16_f32 v194, v148, v149\n"
    "v_mul_f32 v24, v249, v24\n"
    "v_mul_f32 v25, v249, v25\n"
    "v_mfma_f32_16x16x32_bf16 v[44:47], a[176:179], v[184:187], v[44:47]\n"
    "ds_write_b64 v12, v[20:21] offset:4096\n"
    "v_exp_f32 v151, v151\n"
    "v_mul_f32 v26, v249, v26\n"
    "v_mul_f32 v27, v249, v27\n"
    "v_mfma_f32_16x16x32_bf16 v[44:47], a[180:183], v[188:191], v[44:47]\n"
    "ds_read_b64_tr_b16 a[212:213], v3 offset:26112\n"
    "ds_read_b64_tr_b16 a[214:215], v3 offset:30208\n"
    "v_cvt_pk_bf16_f32 v24, v24, v25\n"
    "v_cvt_pk_bf16_f32 v25, v26, v27\n"
    "v_mfma_f32_16x16x32_bf16 v[48:51], a[176:179], v[200:203], v[48:51]\n"
    "ds_read_b64_tr_b16 a[216:217], v4 offset:17920\n"
    "ds_read_b64_tr_b16 a[218:219], v4 offset:22016\n"
    "v_mul_f32 v28, v247, v28\n"
    "v_mul_f32 v29, v247, v29\n"
    "v_mfma_f32_16x16x32_bf16 v[48:51], a[180:183], v[204:207], v[48:51]\n"
    "ds_write_b64 v12, v[24:25] offset:8192\n"
    "v_exp_f32 v120, v120\n"
    "v_mul_f32 v30, v247, v30\n"
    "v_mul_f32 v31, v247, v31\n"
    "v_mfma_f32_16x16x32_bf16 v[52:55], a[184:187], v[168:171], v[52:55]\n"
    "ds_read_b128 a[124:127], v2 offset:26624\n"
    "v_cvt_pk_bf16_f32 v195, v150, v151\n"
    "v_cvt_pk_bf16_f32 v28, v28, v29\n"
    "v_cvt_pk_bf16_f32 v29, v30, v31\n"
    "v_mfma_f32_16x16x32_bf16 v[52:55], a[188:191], v[172:175], v[52:55]\n"
    "ds_write_b64 v13, v[28:29] offset:0\n"
    "v_exp_f32 v121, v121\n"
    "v_mul_f32 v32, v248, v32\n"
    "v_mul_f32 v33, v248, v33\n"
    "v_mfma_f32_16x16x32_bf16 v[56:59], a[184:187], v[184:187], v[56:59]\n"
    "ds_read_b128 a[128:131], v1 offset:20480\n"
    "v_cvt_pk_bf16_f32 v164, v120, v121\n"
    "v_mul_f32 v34, v248, v34\n"
    "v_mul_f32 v35, v248, v35\n"
    "v_mfma_f32_16x16x32_bf16 v[56:59], a[188:191], v[188:191], v[56:59]\n"
    "v_exp_f32 v122, v122\n"
    "v_cvt_pk_bf16_f32 v32, v32, v33\n"
    "v_cvt_pk_bf16_f32 v33, v34, v35\n"
    "v_mfma_f32_16x16x32_bf16 v[60:63], a[184:187], v[200:203], v[60:63]\n"
    "ds_read_b128 a[132:135], v2 offset:20480\n"
    "v_mul_f32 v18, v247, v18\n"
    "v_mul_f32 v36, v249, v36\n"
    "v_mul_f32 v37, v249, v37\n"
    "v_mfma_f32_16x16x32_bf16 v[60:63], a[188:191], v[204:207], v[60:63]\n"
    "s_waitcnt lgkmcnt(5)\n"
    "ds_write_b64 v13, v[32:33] offset:4096\n"
    "v_exp_f32 v123, v123\n"
    "v_mul_f32 v39, v249, v39\n"
    "v_cvt_pk_bf16_f32 v36, v36, v37\n"
    "v_mfma_f32_16x16x32_bf16 v[64:67], a[192:195], v[168:171], v[64:67]\n"
    "ds_read_b64_tr_b16 a[220:221], v4 offset:26112\n"
    "ds_read_b64_tr_b16 a[222:223], v4 offset:30208\n"
    "v_mul_f32 v40, v247, v40\n"
    "v_mul_f32 v41, v247, v41\n"
    "v_mfma_f32_16x16x32_bf16 v[64:67], a[196:199], v[172:175], v[64:67]\n"
    "v_exp_f32 v136, v136\n"
    "v_mul_f32 v42, v247, v42\n"
    "v_mul_f32 v43, v247, v43\n"
    "v_mfma_f32_16x16x32_bf16 v[68:71], a[192:195], v[184:187], v[68:71]\n"
    "ds_read_b128 a[136:139], v1 offset:28672\n"
    "v_cvt_pk_bf16_f32 v165, v122, v123\n"
    "v_cvt_pk_bf16_f32 v40, v40, v41\n"
    "v_cvt_pk_bf16_f32 v41, v42, v43\n"
    "v_mfma_f32_16x16x32_bf16 v[68:71], a[196:199], v[188:191], v[68:71]\n"
    "ds_write_b64 v14, v[40:41] offset:0\n"
    "v_exp_f32 v137, v137\n"
    "v_mul_f32 v45, v248, v45\n"
    "v_mul_f32 v46, v248, v46\n"
    "v_mfma_f32_16x16x32_bf16 v[72:75], a[192:195], v[200:203], v[72:75]\n"
    "ds_read_b128 a[140:143], v2 offset:28672\n"
    "v_cvt_pk_bf16_f32 v180, v136, v137\n"
    "v_mul_f32 v47, v248, v47\n"
    "v_mul_f32 v48, v249, v48\n"
    "v_mfma_f32_16x16x32_bf16 v[72:75], a[196:199], v[204:207], v[72:75]\n"
    "v_exp_f32 v138, v138\n"
    "v_mul_f32 v49, v249, v49\n"
    "v_mul_f32 v50, v249, v50\n"
    "v_mfma_f32_16x16x32_bf16 v[76:79], a[200:203], v[168:171], v[76:79]\n"
    "ds_read_b128 a[144:147], v1 offset:22528\n"
    "v_cvt_pk_bf16_f32 v17, v18, v19\n"
    "v_cvt_pk_bf16_f32 v48, v48, v49\n"
    "v_mul_f32 v52, v247, v52\n"
    "v_mfma_f32_16x16x32_bf16 v[76:79], a[204:207], v[172:175], v[76:79]\n"
    "ds_write_b64 v12, v[16:17] offset:0\n"
    "v_exp_f32 v139, v139\n"
    "v_mul_f32 v53, v247, v53\n"
    "v_mul_f32 v54, v247, v54\n"
    "v_mfma_f32_16x16x32_bf16 v[80:83], a[200:203], v[184:187], v[80:83]\n"
    "ds_read_b128 a[148:151], v2 offset:22528\n"
    "v_cvt_pk_bf16_f32 v181, v138, v139\n"
    "v_cvt_pk_bf16_f32 v52, v52, v53\n"
    "v_mul_f32 v56, v248, v56\n"
    "v_mfma_f32_16x16x32_bf16 v[80:83], a[204:207], v[188:191], v[80:83]\n"
    "v_exp_f32 v152, v152\n"
    "v_mul_f32 v57, v248, v57\n"
    "v_mul_f32 v58, v248, v58\n"
    "v_mfma_f32_16x16x32_bf16 v[84:87], a[200:203], v[200:203], v[84:87]\n"
    "ds_read_b128 a[152:155], v1 offset:30720\n"
    "v_mul_f32 v38, v249, v38\n"
    "v_mul_f32 v59, v248, v59\n"
    "v_cvt_pk_bf16_f32 v56, v56, v57\n"
    "v_mfma_f32_16x16x32_bf16 v[84:87], a[204:207], v[204:207], v[84:87]\n"
    "v_exp_f32 v153, v153\n"
    "v_cvt_pk_bf16_f32 v57, v58, v59\n"
    "v_mul_f32 v60, v249, v60\n"
    "v_mfma_f32_16x16x32_bf16 v[88:91], a[208:211], v[168:171], v[88:91]\n"
    "ds_read_b128 a[156:159], v2 offset:30720\n"
    "v_cvt_pk_bf16_f32 v196, v152, v153\n"
    "v_mul_f32 v63, v249, v63\n"
    "v_mul_f32 v64, v247, v64\n"
    "v_mfma_f32_16x16x32_bf16 v[88:91], a[212:215], v[172:175], v[88:91]\n"
    "ds_write_b64 v15, v[56:57] offset:4096\n"
    "v_exp_f32 v154, v154\n"
    "v_mul_f32 v65, v247, v65\n"
    "v_mul_f32 v66, v247, v66\n"
    "v_mfma_f32_16x16x32_bf16 v[92:95], a[208:211], v[184:187], v[92:95]\n"
    "v_cvt_pk_bf16_f32 v37, v38, v39\n"
    "v_mul_f32 v44, v248, v44\n"
    "v_mul_f32 v67, v247, v67\n"
    "v_cvt_pk_bf16_f32 v64, v64, v65\n"
    "v_mfma_f32_16x16x32_bf16 v[92:95], a[212:215], v[188:191], v[92:95]\n"
    "ds_write_b64 v13, v[36:37] offset:8192\n"
    "v_exp_f32 v155, v155\n"
    "v_cvt_pk_bf16_f32 v65, v66, v67\n"
    "v_mul_f32 v68, v248, v68\n"
    "v_mfma_f32_16x16x32_bf16 v[96:99], a[208:211], v[200:203], v[96:99]\n"
    "ds_write_b64 v12, v[64:65] offset:2048\n"
    "v_cvt_pk_bf16_f32 v197, v154, v155\n"
    "v_cvt_pk_bf16_f32 v44, v44, v45\n"
    "v_mul_f32 v69, v248, v69\n"
    "v_mul_f32 v70, v248, v70\n"
    "v_mfma_f32_16x16x32_bf16 v[96:99], a[212:215], v[204:207], v[96:99]\n"
    "v_cvt_pk_bf16_f32 v45, v46, v47\n"
    "v_mul_f32 v51, v249, v51\n"
    "v_mul_f32 v71, v248, v71\n"
    "v_cvt_pk_bf16_f32 v68, v68, v69\n"
    "v_mfma_f32_16x16x32_bf16 v[100:103], a[216:219], v[168:171], v[100:103]\n"
    "s_waitcnt lgkmcnt(4)\n"
    "ds_write_b64 v14, v[44:45] offset:4096\n"
    "v_exp_f32 v124, v124\n"
    "v_cvt_pk_bf16_f32 v69, v70, v71\n"
    "v_mul_f32 v72, v249, v72\n"
    "v_mfma_f32_16x16x32_bf16 v[100:103], a[220:223], v[172:175], v[100:103]\n"
    "ds_write_b64 v12, v[68:69] offset:6144\n"
    "v_cvt_pk_bf16_f32 v49, v50, v51\n"
    "v_mul_f32 v55, v247, v55\n"
    "v_mul_f32 v73, v249, v73\n"
    "v_mul_f32 v74, v249, v74\n"
    "v_mfma_f32_16x16x32_bf16 v[104:107], a[216:219], v[184:187], v[104:107]\n"
    "ds_write_b64 v14, v[48:49] offset:8192\n"
    "v_exp_f32 v125, v125\n"
    "v_mul_f32 v75, v249, v75\n"
    "v_cvt_pk_bf16_f32 v72, v72, v73\n"
    "v_mfma_f32_16x16x32_bf16 v[104:107], a[220:223], v[188:191], v[104:107]\n"
    "v_cvt_pk_bf16_f32 v166, v124, v125\n"
    "v_cvt_pk_bf16_f32 v53, v54, v55\n"
    "v_cvt_pk_bf16_f32 v73, v74, v75\n"
    "v_mul_f32 v76, v247, v76\n"
    "v_mfma_f32_16x16x32_bf16 v[108:111], a[216:219], v[200:203], v[108:111]\n"
    "s_waitcnt vmcnt(12) lgkmcnt(0)\n"
    "ds_write_b64 v15, v[52:53] offset:0\n"
    "v_exp_f32 v126, v126\n"
    "v_mul_f32 v77, v247, v77\n"
    "v_mul_f32 v78, v247, v78\n"
    "v_mfma_f32_16x16x32_bf16 v[108:111], a[220:223], v[204:207], v[108:111]\n"
    "s_barrier\n"
    "ds_write_b64 v12, v[72:73] offset:10240\n"
    "v_mul_f32 v61, v249, v61\n"
    "v_mul_f32 v62, v249, v62\n"
    "v_mul_f32 v79, v247, v79\n"
    "v_cvt_pk_bf16_f32 v76, v76, v77\n"
    "v_mfma_f32_16x16x32_bf16 v[112:115], a[96:99], a[48:51], v[208:211]\n"
    "v_exp_f32 v127, v127\n"
    "v_cvt_pk_bf16_f32 v77, v78, v79\n"
    "v_mul_f32 v80, v248, v80\n"
    "v_mfma_f32_16x16x32_bf16 v[112:115], a[100:103], a[52:55], v[112:115]\n"
    "s_add_u32 s62, s59, 32768\n"
    "ds_write_b64 v13, v[76:77] offset:2048\n"
    "v_cvt_pk_bf16_f32 v167, v126, v127\n"
    "v_cvt_pk_bf16_f32 v60, v60, v61\n"
    "v_mul_f32 v81, v248, v81\n"
    "v_mul_f32 v82, v248, v82\n"
    "v_mfma_f32_16x16x32_bf16 v[112:115], a[104:107], a[56:59], v[112:115]\n"
    "s_add_u32 m0, s60, 16384\n"
    "v_exp_f32 v140, v140\n"
    "v_mul_f32 v83, v248, v83\n"
    "v_cvt_pk_bf16_f32 v80, v80, v81\n"
    "v_mfma_f32_16x16x32_bf16 v[112:115], a[108:111], a[60:63], v[112:115]\n"
    "s_add_u32 s63, s59, 32896\n"
    "buffer_load_dwordx4 v7, s[44:47], s62 offen nt lds\n"
    "v_cvt_pk_bf16_f32 v81, v82, v83\n"
    "v_mul_f32 v84, v249, v84\n"
    "v_mfma_f32_16x16x32_bf16 v[128:131], a[96:99], a[64:67], v[212:215]\n"
    "s_add_u32 m0, s60, 17408\n"
    "v_exp_f32 v141, v141\n"
    "v_mul_f32 v85, v249, v85\n"
    "v_mul_f32 v86, v249, v86\n"
    "v_mfma_f32_16x16x32_bf16 v[128:131], a[100:103], a[68:71], v[128:131]\n"
    "buffer_load_dwordx4 v7, s[44:47], s63 offen nt lds\n"
    "v_mul_f32 v88, v247, v88\n"
    "v_mul_f32 v89, v247, v89\n"
    "v_mfma_f32_16x16x32_bf16 v[128:131], a[104:107], a[72:75], v[128:131]\n"
    "v_exp_f32 v142, v142\n"
    "v_mul_f32 v90, v247, v90\n"
    "v_mul_f32 v91, v247, v91\n"
    "v_mfma_f32_16x16x32_bf16 v[128:131], a[108:111], a[76:79], v[128:131]\n"
    "s_add_u32 s64, s59, 34816\n"
    "ds_write_b64 v13, v[80:81] offset:6144\n"
    "v_cvt_pk_bf16_f32 v182, v140, v141\n"
    "v_cvt_pk_bf16_f32 v61, v62, v63\n"
    "v_cvt_pk_bf16_f32 v88, v88, v89\n"
    "v_cvt_pk_bf16_f32 v89, v90, v91\n"
    "v_mfma_f32_16x16x32_bf16 v[144:147], a[96:99], a[80:83], v[216:219]\n"
    "s_add_u32 m0, s60, 18432\n"
    "v_exp_f32 v143, v143\n"
    "v_mul_f32 v92, v248, v92\n"
    "v_mul_f32 v93, v248, v93\n"
    "v_mfma_f32_16x16x32_bf16 v[144:147], a[100:103], a[84:87], v[144:147]\n"
    "s_add_u32 s65, s59, 34944\n"
    "buffer_load_dwordx4 v7, s[44:47], s64 offen nt lds\n"
    "v_mul_f32 v94, v248, v94\n"
    "v_mul_f32 v95, v248, v95\n"
    "v_mfma_f32_16x16x32_bf16 v[144:147], a[104:107], a[88:91], v[144:147]\n"
    "s_add_u32 m0, s60, 19456\n"
    "v_exp_f32 v156, v156\n"
    "v_cvt_pk_bf16_f32 v92, v92, v93\n"
    "v_cvt_pk_bf16_f32 v93, v94, v95\n"
    "v_mfma_f32_16x16x32_bf16 v[144:147], a[108:111], a[92:95], v[144:147]\n"
    "buffer_load_dwordx4 v7, s[44:47], s65 offen nt lds\n"
    "v_mul_f32 v96, v249, v96\n"
    "v_mul_f32 v97, v249, v97\n"
    "v_mfma_f32_16x16x32_bf16 v[116:119], a[112:115], a[48:51], v[208:211]\n"
    "v_exp_f32 v157, v157\n"
    "v_mul_f32 v98, v249, v98\n"
    "v_mul_f32 v99, v249, v99\n"
    "v_mfma_f32_16x16x32_bf16 v[116:119], a[116:119], a[52:55], v[116:119]\n"
    "s_add_u32 s62, s57, 81920\n"
    "ds_write_b64 v14, v[88:89] offset:2048\n"
    "v_cvt_pk_bf16_f32 v183, v142, v143\n"
    "v_cvt_pk_bf16_f32 v198, v156, v157\n"
    "v_cvt_pk_bf16_f32 v96, v96, v97\n"
    "v_cvt_pk_bf16_f32 v97, v98, v99\n"
    "v_mfma_f32_16x16x32_bf16 v[116:119], a[120:123], a[56:59], v[116:119]\n"
    "s_add_u32 m0, s58, 16384\n"
    "v_exp_f32 v158, v158\n"
    "v_mul_f32 v100, v247, v100\n"
    "v_mul_f32 v101, v247, v101\n"
    "v_mfma_f32_16x16x32_bf16 v[116:119], a[124:127], a[60:63], v[116:119]\n"
    "buffer_load_dwordx4 v5, s[40:43], s62 offen nt lds\n"
    "v_mul_f32 v102, v247, v102\n"
    "v_mul_f32 v103, v247, v103\n"
    "v_mfma_f32_16x16x32_bf16 v[132:135], a[112:115], a[64:67], v[212:215]\n"
    "s_add_u32 s63, s57, 83968\n"
    "v_mul_f32 v87, v249, v87\n"
    "v_cvt_pk_bf16_f32 v84, v84, v85\n"
    "v_cvt_pk_bf16_f32 v85, v86, v87\n"
    "v_cvt_pk_bf16_f32 v100, v100, v101\n"
    "v_mfma_f32_16x16x32_bf16 v[132:135], a[116:119], a[68:71], v[132:135]\n"
    "s_add_u32 m0, s58, 17408\n"
    "v_exp_f32 v159, v159\n"
    "v_cvt_pk_bf16_f32 v101, v102, v103\n"
    "v_mul_f32 v104, v248, v104\n"
    "v_mfma_f32_16x16x32_bf16 v[132:135], a[120:123], a[72:75], v[132:135]\n"
    "s_add_u32 s64, s57, 86016\n"
    "buffer_load_dwordx4 v6, s[40:43], s63 offen nt lds\n"
    "v_mul_f32 v105, v248, v105\n"
    "v_mul_f32 v106, v248, v106\n"
    "v_mfma_f32_16x16x32_bf16 v[132:135], a[124:127], a[76:79], v[132:135]\n"
    "s_add_u32 m0, s58, 18432\n"
    "v_exp_f32 v112, v112\n"
    "v_mul_f32 v107, v248, v107\n"
    "v_cvt_pk_bf16_f32 v104, v104, v105\n"
    "v_mfma_f32_16x16x32_bf16 v[148:151], a[112:115], a[80:83], v[216:219]\n"
    "buffer_load_dwordx4 v5, s[40:43], s64 offen nt lds\n"
    "v_cvt_pk_bf16_f32 v105, v106, v107\n"
    "v_mul_f32 v108, v249, v108\n"
    "v_mfma_f32_16x16x32_bf16 v[148:151], a[116:119], a[84:87], v[148:151]\n"
    "v_exp_f32 v113, v113\n"
    "v_mul_f32 v109, v249, v109\n"
    "v_mul_f32 v110, v249, v110\n"
    "v_mfma_f32_16x16x32_bf16 v[148:151], a[120:123], a[88:91], v[148:151]\n"
    "s_add_u32 s65, s57, 88064\n"
    "ds_write_b64 v14, v[96:97] offset:10240\n"
    "v_cvt_pk_bf16_f32 v199, v158, v159\n"
    "v_cvt_pk_bf16_f32 v168, v112, v113\n"
    "v_mul_f32 v111, v249, v111\n"
    "v_cvt_pk_bf16_f32 v108, v108, v109\n"
    "v_mfma_f32_16x16x32_bf16 v[148:151], a[124:127], a[92:95], v[148:151]\n"
    "s_add_u32 m0, s58, 19456\n"
    "v_exp_f32 v114, v114\n"
    "v_cvt_pk_bf16_f32 v109, v110, v111\n"
    "v_mfma_f32_16x16x32_bf16 v[120:123], a[128:131], a[48:51], v[208:211]\n"
    "buffer_load_dwordx4 v6, s[40:43], s65 offen nt lds\n"
    "v_mfma_f32_16x16x32_bf16 v[120:123], a[132:135], a[52:55], v[120:123]\n"
    "v_exp_f32 v115, v115\n"
    "v_mfma_f32_16x16x32_bf16 v[120:123], a[136:139], a[56:59], v[120:123]\n"
    "ds_read_b64_tr_b16 a[160:161], v3 offset:32768\n"
    "ds_read_b64_tr_b16 a[162:163], v3 offset:36864\n"
    "v_mfma_f32_16x16x32_bf16 v[120:123], a[140:143], a[60:63], v[120:123]\n"
    "ds_write_b64 v14, v[92:93] offset:6144\n"
    "v_exp_f32 v128, v128\n"
    "v_mfma_f32_16x16x32_bf16 v[136:139], a[128:131], a[64:67], v[212:215]\n"
    "ds_read_b64_tr_b16 a[164:165], v3 offset:40960\n"
    "ds_read_b64_tr_b16 a[166:167], v3 offset:45056\n"
    "v_mfma_f32_16x16x32_bf16 v[136:139], a[132:135], a[68:71], v[136:139]\n"
    "ds_write_b64 v15, v[100:101] offset:2048\n"
    "v_exp_f32 v129, v129\n"
    "v_mfma_f32_16x16x32_bf16 v[136:139], a[136:139], a[72:75], v[136:139]\n"
    "ds_read_b64_tr_b16 a[168:169], v4 offset:32768\n"
    "ds_write_b64 v15, v[104:105] offset:6144\n"
    "v_cvt_pk_bf16_f32 v169, v114, v115\n"
    "v_mfma_f32_16x16x32_bf16 v[136:139], a[140:143], a[76:79], v[136:139]\n"
    "ds_write_b64 v15, v[60:61] offset:8192\n"
    "v_exp_f32 v130, v130\n"
    "v_mfma_f32_16x16x32_bf16 v[152:155], a[128:131], a[80:83], v[216:219]\n"
    "ds_read_b64_tr_b16 a[170:171], v4 offset:36864\n"
    "ds_read_b64_tr_b16 a[172:173], v4 offset:40960\n"
    "v_mfma_f32_16x16x32_bf16 v[152:155], a[132:135], a[84:87], v[152:155]\n"
    "ds_write_b64 v13, v[84:85] offset:10240\n"
    "v_exp_f32 v131, v131\n"
    "v_mfma_f32_16x16x32_bf16 v[152:155], a[136:139], a[88:91], v[152:155]\n"
    "ds_read_b64_tr_b16 a[174:175], v4 offset:45056\n"
    "ds_write_b64 v15, v[108:109] offset:10240\n"
    "v_cvt_pk_bf16_f32 v184, v128, v129\n"
    "v_mfma_f32_16x16x32_bf16 v[152:155], a[140:143], a[92:95], v[152:155]\n"
    "v_exp_f32 v144, v144\n"
    "v_mfma_f32_16x16x32_bf16 v[124:127], a[144:147], a[48:51], v[208:211]\n"
    "ds_read_b64_tr_b16 a[176:177], v3 offset:33280\n"
    "v_cvt_pk_bf16_f32 v185, v130, v131\n"
    "v_mfma_f32_16x16x32_bf16 v[124:127], a[148:151], a[52:55], v[124:127]\n"
    "v_exp_f32 v145, v145\n"
    "v_mfma_f32_16x16x32_bf16 v[124:127], a[152:155], a[56:59], v[124:127]\n"
    "ds_read_b64_tr_b16 a[178:179], v3 offset:37376\n"
    "ds_read_b64_tr_b16 a[180:181], v3 offset:41472\n"
    "v_mfma_f32_16x16x32_bf16 v[124:127], a[156:159], a[60:63], v[124:127]\n"
    "v_exp_f32 v146, v146\n"
    "v_mfma_f32_16x16x32_bf16 v[140:143], a[144:147], a[64:67], v[212:215]\n"
    "ds_read_b64_tr_b16 a[182:183], v3 offset:45568\n"
    "v_cvt_pk_bf16_f32 v200, v144, v145\n"
    "v_mfma_f32_16x16x32_bf16 v[140:143], a[148:151], a[68:71], v[140:143]\n"
    "ds_read_b64_tr_b16 a[184:185], v4 offset:33280\n"
    "ds_read_b64_tr_b16 a[186:187], v4 offset:37376\n"
    "v_mfma_f32_16x16x32_bf16 v[140:143], a[152:155], a[72:75], v[140:143]\n"
    "v_exp_f32 v147, v147\n"
    "v_mfma_f32_16x16x32_bf16 v[140:143], a[156:159], a[76:79], v[140:143]\n"
    "ds_read_b64_tr_b16 a[188:189], v4 offset:41472\n"
    "ds_read_b64_tr_b16 a[190:191], v4 offset:45568\n"
    "v_mfma_f32_16x16x32_bf16 v[156:159], a[144:147], a[80:83], v[216:219]\n"
    "v_exp_f32 v116, v116\n"
    "v_mfma_f32_16x16x32_bf16 v[156:159], a[148:151], a[84:87], v[156:159]\n"
    "ds_read_b64_tr_b16 a[194:195], v3 offset:37888\n"
    "v_cvt_pk_bf16_f32 v201, v146, v147\n"
    "v_mfma_f32_16x16x32_bf16 v[156:159], a[152:155], a[88:91], v[156:159]\n"
    "ds_read_b128 v[232:235], v10 offset:0\n"
    "v_exp_f32 v117, v117\n"
    "v_mfma_f32_16x16x32_bf16 v[156:159], a[156:159], a[92:95], v[156:159]\n"
    "ds_read_b64_tr_b16 a[192:193], v3 offset:33792\n"
    "v_cvt_pk_bf16_f32 v170, v116, v117\n"
    "v_mfma_f32_16x16x32_bf16 a[224:227], a[236:239], v[160:163], 0\n"
    "v_exp_f32 v118, v118\n"
    "v_mfma_f32_16x16x32_bf16 a[224:227], a[236:239], v[164:167], a[224:227]\n"
    "ds_read_b128 a[96:99], v1 offset:32768\n"
    "v_mfma_f32_16x16x32_bf16 a[228:231], a[236:239], v[176:179], 0\n"
    "v_exp_f32 v119, v119\n"
    "v_mfma_f32_16x16x32_bf16 a[228:231], a[236:239], v[180:183], a[228:231]\n"
    "ds_read_b64_tr_b16 a[196:197], v3 offset:41984\n"
    "ds_read_b64_tr_b16 a[198:199], v3 offset:46080\n"
    "v_mfma_f32_16x16x32_bf16 a[232:235], a[236:239], v[192:195], 0\n"
    "s_waitcnt lgkmcnt(4)\n"
    "buffer_store_dwordx4 v[232:235], v11, s[52:55], s72 offen sc1\n"
    "v_exp_f32 v132, v132\n"
    "v_mfma_f32_16x16x32_bf16 a[232:235], a[236:239], v[196:199], a[232:235]\n"
    "ds_read_b128 a[100:103], v2 offset:32768\n"
    "v_cvt_pk_bf16_f32 v171, v118, v119\n"
    "v_mfma_f32_16x16x32_bf16 v[16:19], a[160:163], v[160:163], 0\n"
    "v_exp_f32 v133, v133\n"
    "v_mfma_f32_16x16x32_bf16 v[16:19], a[164:167], v[164:167], v[16:19]\n"
    "ds_read_b128 a[104:107], v1 offset:40960\n"
    "v_cvt_pk_bf16_f32 v186, v132, v133\n"
    "v_mfma_f32_16x16x32_bf16 v[20:23], a[160:163], v[176:179], 0\n"
    "v_exp_f32 v134, v134\n"
    "v_mfma_f32_16x16x32_bf16 v[20:23], a[164:167], v[180:183], v[20:23]\n"
    "ds_read_b64_tr_b16 a[200:201], v4 offset:33792\n"
    "ds_read_b64_tr_b16 a[202:203], v4 offset:37888\n"
    "v_mfma_f32_16x16x32_bf16 v[24:27], a[160:163], v[192:195], 0\n"
    "v_exp_f32 v135, v135\n"
    "v_mfma_f32_16x16x32_bf16 v[24:27], a[164:167], v[196:199], v[24:27]\n"
    "ds_read_b128 a[108:111], v2 offset:40960\n"
    "v_cvt_pk_bf16_f32 v187, v134, v135\n"
    "v_mfma_f32_16x16x32_bf16 v[28:31], a[168:171], v[160:163], 0\n"
    "ds_read_b128 v[236:239], v10 offset:1024\n"
    "v_exp_f32 v148, v148\n"
    "v_mfma_f32_16x16x32_bf16 v[28:31], a[172:175], v[164:167], v[28:31]\n"
    "ds_read_b64_tr_b16 a[204:205], v4 offset:41984\n"
    "ds_read_b64_tr_b16 a[206:207], v4 offset:46080\n"
    "v_mfma_f32_16x16x32_bf16 v[32:35], a[168:171], v[176:179], 0\n"
    "v_exp_f32 v149, v149\n"
    "v_mfma_f32_16x16x32_bf16 v[32:35], a[172:175], v[180:183], v[32:35]\n"
    "ds_read_b128 a[112:115], v1 offset:34816\n"
    "v_cvt_pk_bf16_f32 v202, v148, v149\n"
    "v_mfma_f32_16x16x32_bf16 v[36:39], a[168:171], v[192:195], 0\n"
    "v_exp_f32 v150, v150\n"
    "v_mfma_f32_16x16x32_bf16 v[36:39], a[172:175], v[196:199], v[36:39]\n"
    "ds_read_b128 a[116:119], v2 offset:34816\n"
    "v_mfma_f32_16x16x32_bf16 v[40:43], a[176:179], v[160:163], 0\n"
    "ds_read_b64_tr_b16 a[208:209], v3 offset:34304\n"
    "ds_read_b64_tr_b16 a[210:211], v3 offset:38400\n"
    "v_mfma_f32_16x16x32_bf16 v[40:43], a[180:183], v[164:167], v[40:43]\n"
    "v_exp_f32 v151, v151\n"
    "v_mfma_f32_16x16x32_bf16 v[44:47], a[176:179], v[176:179], 0\n"
    "ds_read_b128 a[120:123], v1 offset:43008\n"
    "v_cvt_pk_bf16_f32 v203, v150, v151\n"
    "v_mfma_f32_16x16x32_bf16 v[44:47], a[180:183], v[180:183], v[44:47]\n"
    "v_exp_f32 v120, v120\n"
    "v_mfma_f32_16x16x32_bf16 v[48:51], a[176:179], v[192:195], 0\n"
    "ds_read_b64_tr_b16 a[212:213], v3 offset:42496\n"
    "ds_read_b64_tr_b16 a[214:215], v3 offset:46592\n"
    "v_mfma_f32_16x16x32_bf16 v[48:51], a[180:183], v[196:199], v[48:51]\n"
    "v_exp_f32 v121, v121\n"
    "v_mfma_f32_16x16x32_bf16 v[52:55], a[184:187], v[160:163], 0\n"
    "ds_read_b128 a[124:127], v2 offset:43008\n"
    "v_cvt_pk_bf16_f32 v172, v120, v121\n"
    "v_mfma_f32_16x16x32_bf16 v[52:55], a[188:191], v[164:167], v[52:55]\n"
    "v_exp_f32 v122, v122\n"
    "v_mfma_f32_16x16x32_bf16 v[56:59], a[184:187], v[176:179], 0\n"
    "s_waitcnt lgkmcnt(4)\n"
    "ds_read_b128 a[128:131], v1 offset:36864\n"
    "buffer_store_dwordx4 v[236:239], v11, s[52:55], s72 offen offset:2048 sc1\n"
    "v_mfma_f32_16x16x32_bf16 v[56:59], a[188:191], v[180:183], v[56:59]\n"
    "v_exp_f32 v123, v123\n"
    "v_mfma_f32_16x16x32_bf16 v[60:63], a[184:187], v[192:195], 0\n"
    "ds_read_b64_tr_b16 a[216:217], v4 offset:34304\n"
    "ds_read_b64_tr_b16 a[218:219], v4 offset:38400\n"
    "v_mfma_f32_16x16x32_bf16 v[60:63], a[188:191], v[196:199], v[60:63]\n"
    "v_exp_f32 v136, v136\n"
    "v_mfma_f32_16x16x32_bf16 v[64:67], a[192:195], v[160:163], 0\n"
    "ds_read_b128 a[132:135], v2 offset:36864\n"
    "v_cvt_pk_bf16_f32 v173, v122, v123\n"
    "v_mfma_f32_16x16x32_bf16 v[64:67], a[196:199], v[164:167], v[64:67]\n"
    "v_exp_f32 v137, v137\n"
    "v_mfma_f32_16x16x32_bf16 v[68:71], a[192:195], v[176:179], 0\n"
    "ds_read_b64_tr_b16 a[220:221], v4 offset:42496\n"
    "ds_read_b64_tr_b16 a[222:223], v4 offset:46592\n"
    "v_mfma_f32_16x16x32_bf16 v[68:71], a[196:199], v[180:183], v[68:71]\n"
    "Lpg5%=:\n"
    "s_cbranch_scc1 Lback%=\n"
    "v_exp_f32 v138, v138\n"
    "v_mfma_f32_16x16x32_bf16 v[72:75], a[192:195], v[192:195], 0\n"
    "ds_read_b128 a[136:139], v1 offset:45056\n"
    "v_cvt_pk_bf16_f32 v188, v136, v137\n"
    "v_mfma_f32_16x16x32_bf16 v[72:75], a[196:199], v[196:199], v[72:75]\n"
    "v_exp_f32 v139, v139\n"
    "v_mfma_f32_16x16x32_bf16 v[76:79], a[200:203], v[160:163], 0\n"
    "ds_read_b128 a[140:143], v2 offset:45056\n"
    "v_cvt_pk_bf16_f32 v189, v138, v139\n"
    "v_mfma_f32_16x16x32_bf16 v[76:79], a[204:207], v[164:167], v[76:79]\n"
    "v_exp_f32 v152, v152\n"
    "v_mfma_f32_16x16x32_bf16 v[80:83], a[200:203], v[176:179], 0\n"
    "ds_read_b128 a[144:147], v1 offset:38912\n"
    "v_mfma_f32_16x16x32_bf16 v[80:83], a[204:207], v[180:183], v[80:83]\n"
    "v_exp_f32 v153, v153\n"
    "v_mfma_f32_16x16x32_bf16 v[84:87], a[200:203], v[192:195], 0\n"
    "ds_read_b128 a[148:151], v2 offset:38912\n"
    "v_cvt_pk_bf16_f32 v204, v152, v153\n"
    "v_mfma_f32_16x16x32_bf16 v[84:87], a[204:207], v[196:199], v[84:87]\n"
    "v_exp_f32 v154, v154\n"
    "v_mfma_f32_16x16x32_bf16 v[88:91], a[208:211], v[160:163], 0\n"
    "s_waitcnt lgkmcnt(3)\n"
    "ds_read_b128 a[152:155], v1 offset:47104\n"
    "v_mfma_f32_16x16x32_bf16 v[88:91], a[212:215], v[164:167], v[88:91]\n"
    "ds_read_b128 a[156:159], v2 offset:47104\n"
    "v_mfma_f32_16x16x32_bf16 v[92:95], a[208:211], v[176:179], 0\n"
    "v_exp_f32 v155, v155\n"
    "v_mfma_f32_16x16x32_bf16 v[92:95], a[212:215], v[180:183], v[92:95]\n"
    "v_cvt_pk_bf16_f32 v205, v154, v155\n"
    "v_mfma_f32_16x16x32_bf16 v[96:99], a[208:211], v[192:195], 0\n"
    "v_exp_f32 v124, v124\n"
    "v_mfma_f32_16x16x32_bf16 v[96:99], a[212:215], v[196:199], v[96:99]\n"
    "v_mfma_f32_16x16x32_bf16 v[100:103], a[216:219], v[160:163], 0\n"
    "v_exp_f32 v125, v125\n"
    "v_mfma_f32_16x16x32_bf16 v[100:103], a[220:223], v[164:167], v[100:103]\n"
    "v_cvt_pk_bf16_f32 v174, v124, v125\n"
    "v_mfma_f32_16x16x32_bf16 v[104:107], a[216:219], v[176:179], 0\n"
    "v_exp_f32 v126, v126\n"
    "v_mfma_f32_16x16x32_bf16 v[104:107], a[220:223], v[180:183], v[104:107]\n"
    "v_mfma_f32_16x16x32_bf16 v[108:111], a[216:219], v[192:195], 0\n"
    "s_waitcnt vmcnt(14) lgkmcnt(0)\n"
    "v_exp_f32 v127, v127\n"
    "v_mfma_f32_16x16x32_bf16 v[108:111], a[220:223], v[196:199], v[108:111]\n"
    "s_barrier\n"
    "v_cvt_pk_bf16_f32 v175, v126, v127\n"
    "v_mfma_f32_16x16x32_bf16 v[112:115], a[96:99], a[48:51], v[208:211]\n"
    "v_exp_f32 v140, v140\n"
    "v_mfma_f32_16x16x32_bf16 v[112:115], a[100:103], a[52:55], v[112:115]\n"
    "s_add_u32 s62, s59, 49152\n"
    "v_mfma_f32_16x16x32_bf16 v[112:115], a[104:107], a[56:59], v[112:115]\n"
    "s_add_u32 m0, s60, 32768\n"
    "v_exp_f32 v141, v141\n"
    "v_mfma_f32_16x16x32_bf16 v[112:115], a[108:111], a[60:63], v[112:115]\n"
    "s_add_u32 s63, s59, 49280\n"
    "buffer_load_dwordx4 v7, s[44:47], s62 offen nt lds\n"
    "v_mfma_f32_16x16x32_bf16 v[128:131], a[96:99], a[64:67], v[212:215]\n"
    "s_add_u32 m0, s60, 33792\n"
    "v_exp_f32 v142, v142\n"
    "v_mfma_f32_16x16x32_bf16 v[128:131], a[100:103], a[68:71], v[128:131]\n"
    "buffer_load_dwordx4 v7, s[44:47], s63 offen nt lds\n"
    "v_mfma_f32_16x16x32_bf16 v[128:131], a[104:107], a[72:75], v[128:131]\n"
    "v_exp_f32 v143, v143\n"
    "v_mfma_f32_16x16x32_bf16 v[128:131], a[108:111], a[76:79], v[128:131]\n"
    "s_add_u32 s64, s59, 51200\n"
    "v_cvt_pk_bf16_f32 v190, v140, v141\n"
    "v_cvt_pk_bf16_f32 v191, v142, v143\n"
    "v_mfma_f32_16x16x32_bf16 v[144:147], a[96:99], a[80:83], v[216:219]\n"
    "s_add_u32 m0, s60, 34816\n"
    "v_exp_f32 v156, v156\n"
    "v_mfma_f32_16x16x32_bf16 v[144:147], a[100:103], a[84:87], v[144:147]\n"
    "s_add_u32 s65, s59, 51328\n"
    "buffer_load_dwordx4 v7, s[44:47], s64 offen nt lds\n"
    "v_mfma_f32_16x16x32_bf16 v[144:147], a[104:107], a[88:91], v[144:147]\n"
    "s_add_u32 m0, s60, 35840\n"
    "v_exp_f32 v157, v157\n"
    "v_mfma_f32_16x16x32_bf16 v[144:147], a[108:111], a[92:95], v[144:147]\n"
    "buffer_load_dwordx4 v7, s[44:47], s65 offen nt lds\n"
    "v_mfma_f32_16x16x32_bf16 v[116:119], a[112:115], a[48:51], v[208:211]\n"
    "s_add_u32 s62, s57, 98304\n"
    "v_exp_f32 v158, v158\n"
    "v_mfma_f32_16x16x32_bf16 v[116:119], a[116:119], a[52:55], v[116:119]\n"
    "s_add_u32 m0, s58, 32768\n"
    "v_cvt_pk_bf16_f32 v206, v156, v157\n"
    "v_mfma_f32_16x16x32_bf16 v[116:119], a[120:123], a[56:59], v[116:119]\n"
    "buffer_load_dwordx4 v5, s[40:43], s62 offen nt lds\n"
    "v_mfma_f32_16x16x32_bf16 v[116:119], a[124:127], a[60:63], v[116:119]\n"
    "v_exp_f32 v159, v159\n"
    "v_mfma_f32_16x16x32_bf16 v[132:135], a[112:115], a[64:67], v[212:215]\n"
    "s_add_u32 s63, s57, 100352\n"
    "ds_read_b128 v[240:243], v10 offset:4096\n"
    "v_cvt_pk_bf16_f32 v207, v158, v159\n"
    "v_mfma_f32_16x16x32_bf16 v[132:135], a[116:119], a[68:71], v[132:135]\n"
    "s_add_u32 m0, s58, 33792\n"
    "v_exp_f32 v112, v112\n"
    "v_mfma_f32_16x16x32_bf16 v[132:135], a[120:123], a[72:75], v[132:135]\n"
    "s_add_u32 s64, s57, 102400\n"
    "buffer_load_dwordx4 v6, s[40:43], s63 offen nt lds\n"
    "v_mfma_f32_16x16x32_bf16 v[132:135], a[124:127], a[76:79], v[132:135]\n"
    "s_add_u32 m0, s58, 34816\n"
    "v_exp_f32 v113, v113\n"
    "v_mfma_f32_16x16x32_bf16 v[148:151], a[112:115], a[80:83], v[216:219]\n"
    "buffer_load_dwordx4 v5, s[40:43], s64 offen nt lds\n"
    "v_mfma_f32_16x16x32_bf16 v[148:151], a[116:119], a[84:87], v[148:151]\n"
    "v_exp_f32 v114, v114\n"
    "v_mfma_f32_16x16x32_bf16 v[148:151], a[120:123], a[88:91], v[148:151]\n"
    "s_add_u32 s65, s57, 104448\n"
    "ds_read_b64_tr_b16 a[160:161], v3 offset:0\n"
    "v_cvt_pk_bf16_f32 v160, v112, v113\n"
    "v_mfma_f32_16x16x32_bf16 v[148:151], a[124:127], a[92:95], v[148:151]\n"
    "s_add_u32 m0, s58, 35840\n"
    "v_exp_f32 v115, v115\n"
    "v_mfma_f32_16x16x32_bf16 v[120:123], a[128:131], a[48:51], v[208:211]\n"
    "buffer_load_dwordx4 v6, s[40:43], s65 offen nt lds\n"
    "v_mfma_f32_16x16x32_bf16 v[120:123], a[132:135], a[52:55], v[120:123]\n"
    "v_exp_f32 v128, v128\n"
    "v_mfma_f32_16x16x32_bf16 v[120:123], a[136:139], a[56:59], v[120:123]\n"
    "ds_read_b64_tr_b16 a[162:163], v3 offset:4096\n"
    "v_cvt_pk_bf16_f32 v161, v114, v115\n"
    "v_mfma_f32_16x16x32_bf16 v[120:123], a[140:143], a[60:63], v[120:123]\n"
    "v_exp_f32 v129, v129\n"
    "v_mfma_f32_16x16x32_bf16 v[136:139], a[128:131], a[64:67], v[212:215]\n"
    "ds_read_b64_tr_b16 a[164:165], v3 offset:8192\n"
    "ds_read_b64_tr_b16 a[166:167], v3 offset:12288\n"
    "v_mfma_f32_16x16x32_bf16 v[136:139], a[132:135], a[68:71], v[136:139]\n"
    "v_exp_f32 v130, v130\n"
    "v_mfma_f32_16x16x32_bf16 v[136:139], a[136:139], a[72:75], v[136:139]\n"
    "ds_read_b64_tr_b16 a[168:169], v4 offset:0\n"
    "v_cvt_pk_bf16_f32 v176, v128, v129\n"
    "v_mfma_f32_16x16x32_bf16 v[136:139], a[140:143], a[76:79], v[136:139]\n"
    "v_exp_f32 v131, v131\n"
    "v_mfma_f32_16x16x32_bf16 v[152:155], a[128:131], a[80:83], v[216:219]\n"
    "ds_read_b64_tr_b16 a[170:171], v4 offset:4096\n"
    "ds_read_b64_tr_b16 a[172:173], v4 offset:8192\n"
    "v_mfma_f32_16x16x32_bf16 v[152:155], a[132:135], a[84:87], v[152:155]\n"
    "v_exp_f32 v144, v144\n"
    "v_mfma_f32_16x16x32_bf16 v[152:155], a[136:139], a[88:91], v[152:155]\n"
    "ds_read_b64_tr_b16 a[174:175], v4 offset:12288\n"
    "v_cvt_pk_bf16_f32 v177, v130, v131\n"
    "v_mfma_f32_16x16x32_bf16 v[152:155], a[140:143], a[92:95], v[152:155]\n"
    "v_exp_f32 v145, v145\n"
    "v_mfma_f32_16x16x32_bf16 v[124:127], a[144:147], a[48:51], v[208:211]\n"
    "s_waitcnt lgkmcnt(4)\n"
    "ds_read_b64_tr_b16 a[176:177], v3 offset:512\n"
    "ds_read_b64_tr_b16 a[178:179], v3 offset:4608\n"
    "buffer_store_dwordx4 v[240:243], v11, s[52:55], s73 offen sc1\n"
    "v_mfma_f32_16x16x32_bf16 v[124:127], a[148:151], a[52:55], v[124:127]\n"
    "ds_read_b64_tr_b16 a[180:181], v3 offset:8704\n"
    "ds_read_b64_tr_b16 a[182:183], v3 offset:12800\n"
    "v_mfma_f32_16x16x32_bf16 v[124:127], a[152:155], a[56:59], v[124:127]\n"
    "v_exp_f32 v146, v146\n"
    "v_mfma_f32_16x16x32_bf16 v[124:127], a[156:159], a[60:63], v[124:127]\n"
    "ds_read_b64_tr_b16 a[186:187], v4 offset:4608\n"
    "v_cvt_pk_bf16_f32 v192, v144, v145\n"
    "v_mfma_f32_16x16x32_bf16 v[140:143], a[144:147], a[64:67], v[212:215]\n"
    "v_exp_f32 v147, v147\n"
    "v_mfma_f32_16x16x32_bf16 v[140:143], a[148:151], a[68:71], v[140:143]\n"
    "ds_read_b64_tr_b16 a[184:185], v4 offset:512\n"
    "v_cvt_pk_bf16_f32 v193, v146, v147\n"
    "v_mfma_f32_16x16x32_bf16 v[140:143], a[152:155], a[72:75], v[140:143]\n"
    "v_exp_f32 v116, v116\n"
    "v_mfma_f32_16x16x32_bf16 v[140:143], a[156:159], a[76:79], v[140:143]\n"
    "ds_read_b64_tr_b16 a[188:189], v4 offset:8704\n"
    "ds_read_b64_tr_b16 a[190:191], v4 offset:12800\n"
    "v_mfma_f32_16x16x32_bf16 v[156:159], a[144:147], a[80:83], v[216:219]\n"
    "v_exp_f32 v117, v117\n"
    "v_mfma_f32_16x16x32_bf16 v[156:159], a[148:151], a[84:87], v[156:159]\n"
    "ds_read_b64_tr_b16 a[196:197], v3 offset:9216\n"
    "v_cvt_pk_bf16_f32 v162, v116, v117\n"
    "v_mfma_f32_16x16x32_bf16 v[156:159], a[152:155], a[88:91], v[156:159]\n"
    "ds_read_b128 v[220:223], v10 offset:5120\n"
    "v_exp_f32 v118, v118\n"
    "v_mfma_f32_16x16x32_bf16 v[156:159], a[156:159], a[92:95], v[156:159]\n"
    "ds_read_b64_tr_b16 a[192:193], v3 offset:1024\n"
    "ds_read_b64_tr_b16 a[194:195], v3 offset:5120\n"
    "v_mfma_f32_16x16x32_bf16 a[224:227], a[236:239], v[168:171], a[224:227]\n"
    "v_exp_f32 v119, v119\n"
    "v_mfma_f32_16x16x32_bf16 a[224:227], a[236:239], v[172:175], a[224:227]\n"
    "ds_read_b128 a[96:99], v1 offset:49152\n"
    "v_cvt_pk_bf16_f32 v163, v118, v119\n"
    "v_mfma_f32_16x16x32_bf16 a[228:231], a[236:239], v[184:187], a[228:231]\n"
    "v_exp_f32 v132, v132\n"
    "v_mfma_f32_16x16x32_bf16 a[228:231], a[236:239], v[188:191], a[228:231]\n"
    "ds_read_b64_tr_b16 a[198:199], v3 offset:13312\n"
    "ds_read_b64_tr_b16 a[200:201], v4 offset:1024\n"
    "v_mfma_f32_16x16x32_bf16 a[232:235], a[236:239], v[200:203], a[232:235]\n"
    "v_exp_f32 v133, v133\n"
    "v_mfma_f32_16x16x32_bf16 a[232:235], a[236:239], v[204:207], a[232:235]\n"
    "ds_read_b128 a[100:103], v2 offset:49152\n"
    "v_cvt_pk_bf16_f32 v178, v132, v133\n"
    "v_mfma_f32_16x16x32_bf16 v[16:19], a[160:163], v[168:171], v[16:19]\n"
    "v_exp_f32 v134, v134\n"
    "v_mfma_f32_16x16x32_bf16 v[16:19], a[164:167], v[172:175], v[16:19]\n"
    "s_waitcnt lgkmcnt(4)\n"
    "ds_read_b128 a[104:107], v1 offset:57344\n"
    "buffer_store_dwordx4 v[220:223], v11, s[52:55], s73 offen offset:2048 sc1\n"
    "v_mfma_f32_16x16x32_bf16 v[20:23], a[160:163], v[184:187], v[20:23]\n"
    "v_exp_f32 v135, v135\n"
    "v_mfma_f32_16x16x32_bf16 v[20:23], a[164:167], v[188:191], v[20:23]\n"
    "ds_read_b64_tr_b16 a[202:203], v4 offset:5120\n"
    "ds_read_b64_tr_b16 a[204:205], v4 offset:9216\n"
    "v_mfma_f32_16x16x32_bf16 v[24:27], a[160:163], v[200:203], v[24:27]\n"
    "v_exp_f32 v148, v148\n"
    "v_mfma_f32_16x16x32_bf16 v[24:27], a[164:167], v[204:207], v[24:27]\n"
    "ds_read_b128 a[108:111], v2 offset:57344\n"
    "v_cvt_pk_bf16_f32 v179, v134, v135\n"
    "v_mfma_f32_16x16x32_bf16 v[28:31], a[168:171], v[168:171], v[28:31]\n"
    "v_exp_f32 v149, v149\n"
    "v_mfma_f32_16x16x32_bf16 v[28:31], a[172:175], v[172:175], v[28:31]\n"
    "ds_read_b64_tr_b16 a[206:207], v4 offset:13312\n"
    "ds_read_b64_tr_b16 a[208:209], v3 offset:1536\n"
    "v_mfma_f32_16x16x32_bf16 v[32:35], a[168:171], v[184:187], v[32:35]\n"
    "ds_read_b128 a[112:115], v1 offset:51200\n"
    "v_cvt_pk_bf16_f32 v194, v148, v149\n"
    "v_mfma_f32_16x16x32_bf16 v[32:35], a[172:175], v[188:191], v[32:35]\n"
    "v_exp_f32 v150, v150\n"
    "v_mfma_f32_16x16x32_bf16 v[36:39], a[168:171], v[200:203], v[36:39]\n"
    "ds_read_b128 a[116:119], v2 offset:51200\n"
    "v_mfma_f32_16x16x32_bf16 v[36:39], a[172:175], v[204:207], v[36:39]\n"
    "v_exp_f32 v151, v151\n"
    "v_mfma_f32_16x16x32_bf16 v[40:43], a[176:179], v[168:171], v[40:43]\n"
    "ds_read_b64_tr_b16 a[210:211], v3 offset:5632\n"
    "ds_read_b64_tr_b16 a[212:213], v3 offset:9728\n"
    "v_mfma_f32_16x16x32_bf16 v[40:43], a[180:183], v[172:175], v[40:43]\n"
    "v_exp_f32 v120, v120\n"
    "v_mfma_f32_16x16x32_bf16 v[44:47], a[176:179], v[184:187], v[44:47]\n"
    "ds_read_b128 a[120:123], v1 offset:59392\n"
    "v_cvt_pk_bf16_f32 v195, v150, v151\n"
    "v_mfma_f32_16x16x32_bf16 v[44:47], a[180:183], v[188:191], v[44:47]\n"
    "v_exp_f32 v121, v121\n"
    "v_mfma_f32_16x16x32_bf16 v[48:51], a[176:179], v[200:203], v[48:51]\n"
    "ds_read_b64_tr_b16 a[214:215], v3 offset:13824\n"
    "ds_read_b64_tr_b16 a[216:217], v4 offset:1536\n"
    "v_mfma_f32_16x16x32_bf16 v[48:51], a[180:183], v[204:207], v[48:51]\n"
    "v_exp_f32 v122, v122\n"
    "v_mfma_f32_16x16x32_bf16 v[52:55], a[184:187], v[168:171], v[52:55]\n"
    "ds_read_b128 a[124:127], v2 offset:59392\n"
    "v_cvt_pk_bf16_f32 v164, v120, v121\n"
    "v_mfma_f32_16x16x32_bf16 v[52:55], a[188:191], v[172:175], v[52:55]\n"
    "v_exp_f32 v123, v123\n"
    "v_mfma_f32_16x16x32_bf16 v[56:59], a[184:187], v[184:187], v[56:59]\n"
    "ds_read_b128 a[128:131], v1 offset:53248\n"
    "v_cvt_pk_bf16_f32 v165, v122, v123\n"
    "v_mfma_f32_16x16x32_bf16 v[56:59], a[188:191], v[188:191], v[56:59]\n"
    "v_exp_f32 v136, v136\n"
    "v_mfma_f32_16x16x32_bf16 v[60:63], a[184:187], v[200:203], v[60:63]\n"
    "ds_read_b64_tr_b16 a[218:219], v4 offset:5632\n"
    "ds_read_b64_tr_b16 a[220:221], v4 offset:9728\n"
    "v_mfma_f32_16x16x32_bf16 v[60:63], a[188:191], v[204:207], v[60:63]\n"
    "v_exp_f32 v137, v137\n"
    "v_mfma_f32_16x16x32_bf16 v[64:67], a[192:195], v[168:171], v[64:67]\n"
    "s_waitcnt lgkmcnt(4)\n"
    "ds_read_b128 a[132:135], v2 offset:53248\n"
    "v_cvt_pk_bf16_f32 v180, v136, v137\n"
    "v_mfma_f32_16x16x32_bf16 v[64:67], a[196:199], v[172:175], v[64:67]\n"
    "v_exp_f32 v138, v138\n"
    "v_mfma_f32_16x16x32_bf16 v[68:71], a[192:195], v[184:187], v[68:71]\n"
    "ds_read_b64_tr_b16 a[222:223], v4 offset:13824\n"
    "v_mfma_f32_16x16x32_bf16 v[68:71], a[196:199], v[188:191], v[68:71]\n"
    "v_exp_f32 v139, v139\n"
    "v_mfma_f32_16x16x32_bf16 v[72:75], a[192:195], v[200:203], v[72:75]\n"
    "ds_read_b128 a[136:139], v1 offset:61440\n"
    "v_cvt_pk_bf16_f32 v181, v138, v139\n"
    "v_mfma_f32_16x16x32_bf16 v[72:75], a[196:199], v[204:207], v[72:75]\n"
    "v_exp_f32 v152, v152\n"
    "v_mfma_f32_16x16x32_bf16 v[76:79], a[200:203], v[168:171], v[76:79]\n"
    "ds_read_b128 a[140:143], v2 offset:61440\n"
    "v_mfma_f32_16x16x32_bf16 v[76:79], a[204:207], v[172:175], v[76:79]\n"
    "v_exp_f32 v153, v153\n"
    "v_mfma_f32_16x16x32_bf16 v[80:83], a[200:203], v[184:187], v[80:83]\n"
    "ds_read_b128 a[144:147], v1 offset:55296\n"
    "v_cvt_pk_bf16_f32 v196, v152, v153\n"
    "v_mfma_f32_16x16x32_bf16 v[80:83], a[204:207], v[188:191], v[80:83]\n"
    "ds_read_b128 a[156:159], v2 offset:63488\n"
    "v_mfma_f32_16x16x32_bf16 v[84:87], a[200:203], v[200:203], v[84:87]\n"
    "v_exp_f32 v154, v154\n"
    "v_mfma_f32_16x16x32_bf16 v[84:87], a[204:207], v[204:207], v[84:87]\n"
    "ds_read_b128 a[148:151], v2 offset:55296\n"
    "v_mfma_f32_16x16x32_bf16 v[88:91], a[208:211], v[168:171], v[88:91]\n"
    "v_exp_f32 v155, v155\n"
    "v_mfma_f32_16x16x32_bf16 v[88:91], a[212:215], v[172:175], v[88:91]\n"
    "ds_read_b128 a[152:155], v1 offset:63488\n"
    "v_cvt_pk_bf16_f32 v197, v154, v155\n"
    "v_mfma_f32_16x16x32_bf16 v[92:95], a[208:211], v[184:187], v[92:95]\n"
    "v_exp_f32 v124, v124\n"
    "v_mfma_f32_16x16x32_bf16 v[92:95], a[212:215], v[188:191], v[92:95]\n"
    "v_mfma_f32_16x16x32_bf16 v[96:99], a[208:211], v[200:203], v[96:99]\n"
    "v_exp_f32 v125, v125\n"
    "v_mfma_f32_16x16x32_bf16 v[96:99], a[212:215], v[204:207], v[96:99]\n"
    "s_waitcnt lgkmcnt(2)\n"
    "v_cvt_pk_bf16_f32 v166, v124, v125\n"
    "v_mfma_f32_16x16x32_bf16 v[100:103], a[216:219], v[168:171], v[100:103]\n"
    "v_exp_f32 v126, v126\n"
    "v_mfma_f32_16x16x32_bf16 v[100:103], a[220:223], v[172:175], v[100:103]\n"
    "v_mfma_f32_16x16x32_bf16 v[104:107], a[216:219], v[184:187], v[104:107]\n"
    "v_exp_f32 v127, v127\n"
    "v_mfma_f32_16x16x32_bf16 v[104:107], a[220:223], v[188:191], v[104:107]\n"
    "v_cvt_pk_bf16_f32 v167, v126, v127\n"
    "v_mfma_f32_16x16x32_bf16 v[108:111], a[216:219], v[200:203], v[108:111]\n"
    "s_waitcnt vmcnt(16) lgkmcnt(0)\n"
    "v_exp_f32 v140, v140\n"
    "v_mfma_f32_16x16x32_bf16 v[108:111], a[220:223], v[204:207], v[108:111]\n"
    "s_barrier\n"
    "v_mfma_f32_16x16x32_bf16 v[112:115], a[96:99], a[48:51], v[208:211]\n"
    "v_exp_f32 v141, v141\n"
    "v_mfma_f32_16x16x32_bf16 v[112:115], a[100:103], a[52:55], v[112:115]\n"
    "s_add_u32 s62, s59, 65536\n"
    "v_cvt_pk_bf16_f32 v182, v140, v141\n"
    "v_mfma_f32_16x16x32_bf16 v[112:115], a[104:107], a[56:59], v[112:115]\n"
    "s_add_u32 m0, s60, 0\n"
    "v_exp_f32 v142, v142\n"
    "v_mfma_f32_16x16x32_bf16 v[112:115], a[108:111], a[60:63], v[112:115]\n"
    "buffer_load_dwordx4 v7, s[44:47], s62 offen nt lds\n"
    "v_mfma_f32_16x16x32_bf16 v[128:131], a[96:99], a[64:67], v[212:215]\n"
    "v_exp_f32 v143, v143\n"
    "v_mfma_f32_16x16x32_bf16 v[128:131], a[100:103], a[68:71], v[128:131]\n"
    "v_cvt_pk_bf16_f32 v183, v142, v143\n"
    "v_mfma_f32_16x16x32_bf16 v[128:131], a[104:107], a[72:75], v[128:131]\n"
    "v_exp_f32 v156, v156\n"
    "v_mfma_f32_16x16x32_bf16 v[128:131], a[108:111], a[76:79], v[128:131]\n"
    "v_mfma_f32_16x16x32_bf16 v[144:147], a[96:99], a[80:83], v[216:219]\n"
    "v_exp_f32 v157, v157\n"
    "v_mfma_f32_16x16x32_bf16 v[144:147], a[100:103], a[84:87], v[144:147]\n"
    "v_cvt_pk_bf16_f32 v198, v156, v157\n"
    "v_mfma_f32_16x16x32_bf16 v[144:147], a[104:107], a[88:91], v[144:147]\n"
    "v_mfma_f32_16x16x32_bf16 v[144:147], a[108:111], a[92:95], v[144:147]\n"
    "v_exp_f32 v158, v158\n"
    "v_mfma_f32_16x16x32_bf16 v[116:119], a[112:115], a[48:51], v[208:211]\n"
    "s_add_u32 s63, s59, 65664\n"
    "v_mfma_f32_16x16x32_bf16 v[116:119], a[116:119], a[52:55], v[116:119]\n"
    "s_add_u32 m0, s60, 1024\n"
    "v_exp_f32 v159, v159\n"
    "v_mfma_f32_16x16x32_bf16 v[116:119], a[120:123], a[56:59], v[116:119]\n"
    "buffer_load_dwordx4 v7, s[44:47], s63 offen nt lds\n"
    "v_mfma_f32_16x16x32_bf16 v[116:119], a[124:127], a[60:63], v[116:119]\n"
    "v_exp_f32 v112, v112\n"
    "v_mfma_f32_16x16x32_bf16 v[132:135], a[112:115], a[64:67], v[212:215]\n"
    "ds_read_b128 v[224:227], v10 offset:8192\n"
    "v_cvt_pk_bf16_f32 v199, v158, v159\n"
    "v_mfma_f32_16x16x32_bf16 v[132:135], a[116:119], a[68:71], v[132:135]\n"
    "v_exp_f32 v113, v113\n"
    "v_mfma_f32_16x16x32_bf16 v[132:135], a[120:123], a[72:75], v[132:135]\n"
    "v_cvt_pk_bf16_f32 v168, v112, v113\n"
    "v_mfma_f32_16x16x32_bf16 v[132:135], a[124:127], a[76:79], v[132:135]\n"
    "v_exp_f32 v114, v114\n"
    "v_mfma_f32_16x16x32_bf16 v[148:151], a[112:115], a[80:83], v[216:219]\n"
    "ds_read_b64_tr_b16 a[166:167], v3 offset:28672\n"
    "v_mfma_f32_16x16x32_bf16 v[148:151], a[116:119], a[84:87], v[148:151]\n"
    "v_exp_f32 v115, v115\n"
    "v_mfma_f32_16x16x32_bf16 v[148:151], a[120:123], a[88:91], v[148:151]\n"
    "s_waitcnt lgkmcnt(1)\n"
    "ds_read_b64_tr_b16 a[160:161], v3 offset:16384\n"
    "buffer_store_dwordx4 v[224:227], v11, s[52:55], s74 offen sc1\n"
    "v_cvt_pk_bf16_f32 v169, v114, v115\n"
    "v_mfma_f32_16x16x32_bf16 v[148:151], a[124:127], a[92:95], v[148:151]\n"
    "v_exp_f32 v128, v128\n"
    "v_mfma_f32_16x16x32_bf16 v[120:123], a[128:131], a[48:51], v[208:211]\n"
    "s_add_u32 s64, s59, 67584\n"
    "ds_read_b64_tr_b16 a[162:163], v3 offset:20480\n"
    "ds_read_b64_tr_b16 a[164:165], v3 offset:24576\n"
    "v_mfma_f32_16x16x32_bf16 v[120:123], a[132:135], a[52:55], v[120:123]\n"
    "s_add_u32 m0, s60, 2048\n"
    "v_exp_f32 v129, v129\n"
    "v_mfma_f32_16x16x32_bf16 v[120:123], a[136:139], a[56:59], v[120:123]\n"
    "buffer_load_dwordx4 v7, s[44:47], s64 offen nt lds\n"
    "v_mfma_f32_16x16x32_bf16 v[120:123], a[140:143], a[60:63], v[120:123]\n"
    "v_exp_f32 v130, v130\n"
    "v_mfma_f32_16x16x32_bf16 v[136:139], a[128:131], a[64:67], v[212:215]\n"
    "ds_read_b64_tr_b16 a[168:169], v4 offset:16384\n"
    "v_cvt_pk_bf16_f32 v184, v128, v129\n"
    "v_mfma_f32_16x16x32_bf16 v[136:139], a[132:135], a[68:71], v[136:139]\n"
    "v_exp_f32 v131, v131\n"
    "v_mfma_f32_16x16x32_bf16 v[136:139], a[136:139], a[72:75], v[136:139]\n"
    "ds_read_b64_tr_b16 a[170:171], v4 offset:20480\n"
    "ds_read_b64_tr_b16 a[172:173], v4 offset:24576\n"
    "v_mfma_f32_16x16x32_bf16 v[136:139], a[140:143], a[76:79], v[136:139]\n"
    "v_exp_f32 v144, v144\n"
    "v_mfma_f32_16x16x32_bf16 v[152:155], a[128:131], a[80:83], v[216:219]\n"
    "ds_read_b64_tr_b16 a[174:175], v4 offset:28672\n"
    "v_cvt_pk_bf16_f32 v185, v130, v131\n"
    "v_mfma_f32_16x16x32_bf16 v[152:155], a[132:135], a[84:87], v[152:155]\n"
    "ds_read_b128 v[228:231], v10 offset:9216\n"
    "v_exp_f32 v145, v145\n"
    "v_mfma_f32_16x16x32_bf16 v[152:155], a[136:139], a[88:91], v[152:155]\n"
    "ds_read_b64_tr_b16 a[180:181], v3 offset:25088\n"
    "v_cvt_pk_bf16_f32 v200, v144, v145\n"
    "v_mfma_f32_16x16x32_bf16 v[152:155], a[140:143], a[92:95], v[152:155]\n"
    "s_add_u32 s65, s59, 67712\n"
    "ds_read_b64_tr_b16 a[176:177], v3 offset:16896\n"
    "ds_read_b64_tr_b16 a[178:179], v3 offset:20992\n"
    "v_mfma_f32_16x16x32_bf16 v[124:127], a[144:147], a[48:51], v[208:211]\n"
    "s_add_u32 m0, s60, 3072\n"
    "v_exp_f32 v146, v146\n"
    "v_mfma_f32_16x16x32_bf16 v[124:127], a[148:151], a[52:55], v[124:127]\n"
    "buffer_load_dwordx4 v7, s[44:47], s65 offen nt lds\n"
    "v_mfma_f32_16x16x32_bf16 v[124:127], a[152:155], a[56:59], v[124:127]\n"
    "v_exp_f32 v147, v147\n"
    "v_mfma_f32_16x16x32_bf16 v[124:127], a[156:159], a[60:63], v[124:127]\n"
    "ds_read_b64_tr_b16 a[182:183], v3 offset:29184\n"
    "v_cvt_pk_bf16_f32 v201, v146, v147\n"
    "v_mfma_f32_16x16x32_bf16 v[140:143], a[144:147], a[64:67], v[212:215]\n"
    "s_waitcnt lgkmcnt(4)\n"
    "buffer_store_dwordx4 v[228:231], v11, s[52:55], s74 offen offset:2048 sc1\n"
    "v_exp_f32 v116, v116\n"
    "v_mfma_f32_16x16x32_bf16 v[140:143], a[148:151], a[68:71], v[140:143]\n"
    "ds_read_b64_tr_b16 a[184:185], v4 offset:16896\n"
    "ds_read_b64_tr_b16 a[186:187], v4 offset:20992\n"
    "v_mfma_f32_16x16x32_bf16 v[140:143], a[152:155], a[72:75], v[140:143]\n"
    "v_exp_f32 v117, v117\n"
    "v_mfma_f32_16x16x32_bf16 v[140:143], a[156:159], a[76:79], v[140:143]\n"
    "ds_read_b64_tr_b16 a[188:189], v4 offset:25088\n"
    "ds_read_b64_tr_b16 a[190:191], v4 offset:29184\n"
    "v_mfma_f32_16x16x32_bf16 v[156:159], a[144:147], a[80:83], v[216:219]\n"
    "v_exp_f32 v118, v118\n"
    "v_mfma_f32_16x16x32_bf16 v[156:159], a[148:151], a[84:87], v[156:159]\n"
    "ds_read_b128 a[96:99], v1 offset:0\n"
    "v_cvt_pk_bf16_f32 v170, v116, v117\n"
    "v_mfma_f32_16x16x32_bf16 v[156:159], a[152:155], a[88:91], v[156:159]\n"
    "v_exp_f32 v119, v119\n"
    "v_mfma_f32_16x16x32_bf16 v[156:159], a[156:159], a[92:95], v[156:159]\n"
    "s_add_u32 s62, s57, 114688\n"
    "ds_read_b64_tr_b16 a[192:193], v3 offset:17408\n"
    "ds_read_b64_tr_b16 a[194:195], v3 offset:21504\n"
    "v_mfma_f32_16x16x32_bf16 a[224:227], a[236:239], v[160:163], a[224:227]\n"
    "s_add_u32 m0, s58, 49152\n"
    "v_exp_f32 v132, v132\n"
    "v_mfma_f32_16x16x32_bf16 a[224:227], a[236:239], v[164:167], a[224:227]\n"
    "buffer_load_dwordx4 v5, s[40:43], s62 offen nt lds\n"
    "v_mfma_f32_16x16x32_bf16 a[228:231], a[236:239], v[176:179], a[228:231]\n"
    "v_exp_f32 v133, v133\n"
    "v_mfma_f32_16x16x32_bf16 a[228:231], a[236:239], v[180:183], a[228:231]\n"
    "ds_read_b64_tr_b16 a[196:197], v3 offset:25600\n"
    "v_cvt_pk_bf16_f32 v171, v118, v119\n"
    "v_mfma_f32_16x16x32_bf16 a[232:235], a[236:239], v[192:195], a[232:235]\n"
    "v_exp_f32 v134, v134\n"
    "v_mfma_f32_16x16x32_bf16 a[232:235], a[236:239], v[196:199], a[232:235]\n"
    "ds_read_b64_tr_b16 a[198:199], v3 offset:29696\n"
    "v_cvt_pk_bf16_f32 v186, v132, v133\n"
    "v_mfma_f32_16x16x32_bf16 v[16:19], a[160:163], v[160:163], v[16:19]\n"
    "v_exp_f32 v135, v135\n"
    "v_mfma_f32_16x16x32_bf16 v[16:19], a[164:167], v[164:167], v[16:19]\n"
    "ds_read_b128 a[100:103], v2 offset:0\n"
    "v_cvt_pk_bf16_f32 v187, v134, v135\n"
    "v_mfma_f32_16x16x32_bf16 v[20:23], a[160:163], v[176:179], v[20:23]\n"
    "v_exp_f32 v148, v148\n"
    "v_mfma_f32_16x16x32_bf16 v[20:23], a[164:167], v[180:183], v[20:23]\n"
    "ds_read_b128 a[104:107], v1 offset:8192\n"
    "v_mfma_f32_16x16x32_bf16 v[24:27], a[160:163], v[192:195], v[24:27]\n"
    "s_add_u32 s63, s57, 116736\n"
    "ds_read_b64_tr_b16 a[200:201], v4 offset:17408\n"
    "ds_read_b64_tr_b16 a[202:203], v4 offset:21504\n"
    "v_mfma_f32_16x16x32_bf16 v[24:27], a[164:167], v[196:199], v[24:27]\n"
    "s_add_u32 m0, s58, 50176\n"
    "v_exp_f32 v149, v149\n"
    "v_mfma_f32_16x16x32_bf16 v[28:31], a[168:171], v[160:163], v[28:31]\n"
    "buffer_load_dwordx4 v6, s[40:43], s63 offen nt lds\n"
    "ds_read_b128 a[156:159], v2 offset:14336\n"
    "v_mfma_f32_16x16x32_bf16 v[28:31], a[172:175], v[164:167], v[28:31]\n"
    "ds_read_b128 a[152:155], v1 offset:14336\n"
    "v_exp_f32 v150, v150\n"
    "v_mfma_f32_16x16x32_bf16 v[32:35], a[168:171], v[176:179], v[32:35]\n"
    "ds_read_b128 a[108:111], v2 offset:8192\n"
    "v_cvt_pk_bf16_f32 v202, v148, v149\n"
    "v_mfma_f32_16x16x32_bf16 v[32:35], a[172:175], v[180:183], v[32:35]\n"
    "v_exp_f32 v151, v151\n"
    "v_mfma_f32_16x16x32_bf16 v[36:39], a[168:171], v[192:195], v[36:39]\n"
    "ds_read_b64_tr_b16 a[204:205], v4 offset:25600\n"
    "ds_read_b64_tr_b16 a[206:207], v4 offset:29696\n"
    "v_mfma_f32_16x16x32_bf16 v[36:39], a[172:175], v[196:199], v[36:39]\n"
    "s_waitcnt lgkmcnt(5)\n"
    "v_exp_f32 v120, v120\n"
    "v_mfma_f32_16x16x32_bf16 v[40:43], a[176:179], v[160:163], v[40:43]\n"
    "ds_read_b128 a[112:115], v1 offset:2048\n"
    "v_cvt_pk_bf16_f32 v203, v150, v151\n"
    "v_mfma_f32_16x16x32_bf16 v[40:43], a[180:183], v[164:167], v[40:43]\n"
    "v_exp_f32 v121, v121\n"
    "v_mfma_f32_16x16x32_bf16 v[44:47], a[176:179], v[176:179], v[44:47]\n"
    "ds_read_b128 a[116:119], v2 offset:2048\n"
    "v_cvt_pk_bf16_f32 v172, v120, v121\n"
    "v_mfma_f32_16x16x32_bf16 v[44:47], a[180:183], v[180:183], v[44:47]\n"
    "v_exp_f32 v122, v122\n"
    "v_mfma_f32_16x16x32_bf16 v[48:51], a[176:179], v[192:195], v[48:51]\n"
    "s_add_u32 s64, s57, 118784\n"
    "ds_read_b64_tr_b16 a[208:209], v3 offset:17920\n"
    "ds_read_b64_tr_b16 a[210:211], v3 offset:22016\n"
    "v_mfma_f32_16x16x32_bf16 v[48:51], a[180:183], v[196:199], v[48:51]\n"
    "s_add_u32 m0, s58, 51200\n"
    "v_exp_f32 v123, v123\n"
    "v_mfma_f32_16x16x32_bf16 v[52:55], a[184:187], v[160:163], v[52:55]\n"
    "buffer_load_dwordx4 v5, s[40:43], s64 offen nt lds\n"
    "ds_read_b128 a[144:147], v1 offset:6144\n"
    "v_mfma_f32_16x16x32_bf16 v[52:55], a[188:191], v[164:167], v[52:55]\n"
    "v_exp_f32 v136, v136\n"
    "v_mfma_f32_16x16x32_bf16 v[56:59], a[184:187], v[176:179], v[56:59]\n"
    "ds_read_b128 a[120:123], v1 offset:10240\n"
    "v_cvt_pk_bf16_f32 v173, v122, v123\n"
    "v_mfma_f32_16x16x32_bf16 v[56:59], a[188:191], v[180:183], v[56:59]\n"
    "v_exp_f32 v137, v137\n"
    "v_mfma_f32_16x16x32_bf16 v[60:63], a[184:187], v[192:195], v[60:63]\n"
    "ds_read_b64_tr_b16 a[212:213], v3 offset:26112\n"
    "ds_read_b64_tr_b16 a[214:215], v3 offset:30208\n"
    "v_mfma_f32_16x16x32_bf16 v[60:63], a[188:191], v[196:199], v[60:63]\n"
    "v_exp_f32 v138, v138\n"
    "v_mfma_f32_16x16x32_bf16 v[64:67], a[192:195], v[160:163], v[64:67]\n"
    "ds_read_b128 a[124:127], v2 offset:10240\n"
    "v_cvt_pk_bf16_f32 v188, v136, v137\n"
    "v_mfma_f32_16x16x32_bf16 v[64:67], a[196:199], v[164:167], v[64:67]\n"
    "v_exp_f32 v139, v139\n"
    "v_mfma_f32_16x16x32_bf16 v[68:71], a[192:195], v[176:179], v[68:71]\n"
    "ds_read_b128 a[128:131], v1 offset:4096\n"
    "v_cvt_pk_bf16_f32 v189, v138, v139\n"
    "v_mfma_f32_16x16x32_bf16 v[68:71], a[196:199], v[180:183], v[68:71]\n"
    "v_exp_f32 v152, v152\n"
    "v_mfma_f32_16x16x32_bf16 v[72:75], a[192:195], v[192:195], v[72:75]\n"
    "s_add_u32 s65, s57, 120832\n"
    "ds_read_b64_tr_b16 a[216:217], v4 offset:17920\n"
    "ds_read_b64_tr_b16 a[218:219], v4 offset:22016\n"
    "v_mfma_f32_16x16x32_bf16 v[72:75], a[196:199], v[196:199], v[72:75]\n"
    "s_add_u32 m0, s58, 52224\n"
    "ds_read_b64_tr_b16 a[220:221], v4 offset:26112\n"
    "ds_read_b64_tr_b16 a[222:223], v4 offset:30208\n"
    "v_mfma_f32_16x16x32_bf16 v[76:79], a[200:203], v[160:163], v[76:79]\n"
    "s_waitcnt lgkmcnt(6)\n"
    "v_exp_f32 v153, v153\n"
    "v_mfma_f32_16x16x32_bf16 v[76:79], a[204:207], v[164:167], v[76:79]\n"
    "buffer_load_dwordx4 v6, s[40:43], s65 offen nt lds\n"
    "ds_read_b128 a[148:151], v2 offset:6144\n"
    "v_mfma_f32_16x16x32_bf16 v[80:83], a[200:203], v[176:179], v[80:83]\n"
    "v_exp_f32 v154, v154\n"
    "v_mfma_f32_16x16x32_bf16 v[80:83], a[204:207], v[180:183], v[80:83]\n"
    "ds_read_b128 a[132:135], v2 offset:4096\n"
    "v_cvt_pk_bf16_f32 v204, v152, v153\n"
    "v_mfma_f32_16x16x32_bf16 v[84:87], a[200:203], v[192:195], v[84:87]\n"
    "v_exp_f32 v155, v155\n"
    "v_mfma_f32_16x16x32_bf16 v[84:87], a[204:207], v[196:199], v[84:87]\n"
    "ds_read_b128 a[136:139], v1 offset:12288\n"
    "v_cvt_pk_bf16_f32 v205, v154, v155\n"
    "v_mfma_f32_16x16x32_bf16 v[88:91], a[208:211], v[160:163], v[88:91]\n"
    "v_exp_f32 v124, v124\n"
    "v_mfma_f32_16x16x32_bf16 v[88:91], a[212:215], v[164:167], v[88:91]\n"
    "ds_read_b128 a[140:143], v2 offset:12288\n"
    "v_mfma_f32_16x16x32_bf16 v[92:95], a[208:211], v[176:179], v[92:95]\n"
    "v_exp_f32 v125, v125\n"
    "v_mfma_f32_16x16x32_bf16 v[92:95], a[212:215], v[180:183], v[92:95]\n"
    "v_cvt_pk_bf16_f32 v174, v124, v125\n"
    "v_mfma_f32_16x16x32_bf16 v[96:99], a[208:211], v[192:195], v[96:99]\n"
    "v_exp_f32 v126, v126\n"
    "v_mfma_f32_16x16x32_bf16 v[96:99], a[212:215], v[196:199], v[96:99]\n"
    "s_waitcnt lgkmcnt(2)\n"
    "v_mfma_f32_16x16x32_bf16 v[100:103], a[216:219], v[160:163], v[100:103]\n"
    "v_exp_f32 v127, v127\n"
    "v_mfma_f32_16x16x32_bf16 v[100:103], a[220:223], v[164:167], v[100:103]\n"
    "v_cvt_pk_bf16_f32 v175, v126, v127\n"
    "v_mfma_f32_16x16x32_bf16 v[104:107], a[216:219], v[176:179], v[104:107]\n"
    "v_exp_f32 v140, v140\n"
    "v_mfma_f32_16x16x32_bf16 v[104:107], a[220:223], v[180:183], v[104:107]\n"
    "v_mfma_f32_16x16x32_bf16 v[108:111], a[216:219], v[192:195], v[108:111]\n"
    "s_waitcnt vmcnt(16) lgkmcnt(0)\n"
    "v_exp_f32 v141, v141\n"
    "v_mfma_f32_16x16x32_bf16 v[108:111], a[220:223], v[196:199], v[108:111]\n"
    "s_barrier\n"
    "v_cvt_pk_bf16_f32 v190, v140, v141\n"
    "v_mfma_f32_16x16x32_bf16 v[112:115], a[96:99], a[48:51], v[208:211]\n"
    "v_exp_f32 v142, v142\n"
    "v_mfma_f32_16x16x32_bf16 v[112:115], a[100:103], a[52:55], v[112:115]\n"
    "s_add_u32 s62, s59, 81920\n"
    "ds_read_b128 v[232:235], v10 offset:2048\n"
    "v_mfma_f32_16x16x32_bf16 v[112:115], a[104:107], a[56:59], v[112:115]\n"
    "s_add_u32 m0, s60, 16384\n"
    "v_exp_f32 v143, v143\n"
    "v_mfma_f32_16x16x32_bf16 v[112:115], a[108:111], a[60:63], v[112:115]\n"
    "buffer_load_dwordx4 v7, s[44:47], s62 offen nt lds\n"
    "v_mfma_f32_16x16x32_bf16 v[128:131], a[96:99], a[64:67], v[212:215]\n"
    "v_exp_f32 v156, v156\n"
    "v_mfma_f32_16x16x32_bf16 v[128:131], a[100:103], a[68:71], v[128:131]\n"
    "Lpg6%=:\n"
    "s_cbranch_scc1 Lback%=\n"
    "v_cvt_pk_bf16_f32 v191, v142, v143\n"
    "v_mfma_f32_16x16x32_bf16 v[128:131], a[104:107], a[72:75], v[128:131]\n"
    "v_mfma_f32_16x16x32_bf16 v[128:131], a[108:111], a[76:79], v[128:131]\n"
    "v_exp_f32 v157, v157\n"
    "v_mfma_f32_16x16x32_bf16 v[144:147], a[96:99], a[80:83], v[216:219]\n"
    "v_cvt_pk_bf16_f32 v206, v156, v157\n"
    "v_mfma_f32_16x16x32_bf16 v[144:147], a[100:103], a[84:87], v[144:147]\n"
    "v_exp_f32 v158, v158\n"
    "v_mfma_f32_16x16x32_bf16 v[144:147], a[104:107], a[88:91], v[144:147]\n"
    "s_waitcnt lgkmcnt(0)\n"
    "buffer_store_dwordx4 v[232:235], v11, s[52:55], s72 offen offset:128 sc1\n"
    "v_mfma_f32_16x16x32_bf16 v[144:147], a[108:111], a[92:95], v[144:147]\n"
    "v_exp_f32 v159, v159\n"
    "v_mfma_f32_16x16x32_bf16 v[116:119], a[112:115], a[48:51], v[208:211]\n"
    "v_cvt_pk_bf16_f32 v207, v158, v159\n"
    "v_mfma_f32_16x16x32_bf16 v[116:119], a[116:119], a[52:55], v[116:119]\n"
    "v_exp_f32 v112, v112\n"
    "v_mfma_f32_16x16x32_bf16 v[116:119], a[120:123], a[56:59], v[116:119]\n"
    "v_mfma_f32_16x16x32_bf16 v[116:119], a[124:127], a[60:63], v[116:119]\n"
    "v_exp_f32 v113, v113\n"
    "v_mfma_f32_16x16x32_bf16 v[132:135], a[112:115], a[64:67], v[212:215]\n"
    "v_cvt_pk_bf16_f32 v160, v112, v113\n"
    "v_mfma_f32_16x16x32_bf16 v[132:135], a[116:119], a[68:71], v[132:135]\n"
    "v_exp_f32 v114, v114\n"
    "v_mfma_f32_16x16x32_bf16 v[132:135], a[120:123], a[72:75], v[132:135]\n"
    "v_mfma_f32_16x16x32_bf16 v[132:135], a[124:127], a[76:79], v[132:135]\n"
    "v_exp_f32 v115, v115\n"
    "v_mfma_f32_16x16x32_bf16 v[148:151], a[112:115], a[80:83], v[216:219]\n"
    "ds_read_b128 v[236:239], v10 offset:3072\n"
    "v_cvt_pk_bf16_f32 v161, v114, v115\n"
    "v_mfma_f32_16x16x32_bf16 v[148:151], a[116:119], a[84:87], v[148:151]\n"
    "v_exp_f32 v128, v128\n"
    "v_mfma_f32_16x16x32_bf16 v[148:151], a[120:123], a[88:91], v[148:151]\n"
    "ds_read_b64_tr_b16 a[160:161], v3 offset:32768\n"
    "ds_read_b64_tr_b16 a[162:163], v3 offset:36864\n"
    "v_mfma_f32_16x16x32_bf16 v[148:151], a[124:127], a[92:95], v[148:151]\n"
    "v_exp_f32 v129, v129\n"
    "v_mfma_f32_16x16x32_bf16 v[120:123], a[128:131], a[48:51], v[208:211]\n"
    "s_add_u32 s63, s59, 82048\n"
    "ds_read_b64_tr_b16 a[164:165], v3 offset:40960\n"
    "ds_read_b64_tr_b16 a[166:167], v3 offset:45056\n"
    "v_mfma_f32_16x16x32_bf16 v[120:123], a[132:135], a[52:55], v[120:123]\n"
    "s_add_u32 m0, s60, 17408\n"
    "v_exp_f32 v130, v130\n"
    "v_mfma_f32_16x16x32_bf16 v[120:123], a[136:139], a[56:59], v[120:123]\n"
    "buffer_load_dwordx4 v7, s[44:47], s63 offen nt lds\n"
    "v_mfma_f32_16x16x32_bf16 v[120:123], a[140:143], a[60:63], v[120:123]\n"
    "v_exp_f32 v131, v131\n"
    "v_mfma_f32_16x16x32_bf16 v[136:139], a[128:131], a[64:67], v[212:215]\n"
    "v_cvt_pk_bf16_f32 v176, v128, v129\n"
    "v_cvt_pk_bf16_f32 v177, v130, v131\n"
    "v_mfma_f32_16x16x32_bf16 v[136:139], a[132:135], a[68:71], v[136:139]\n"
    "v_exp_f32 v144, v144\n"
    "v_mfma_f32_16x16x32_bf16 v[136:139], a[136:139], a[72:75], v[136:139]\n"
    "ds_read_b64_tr_b16 a[168:169], v4 offset:32768\n"
    "ds_read_b64_tr_b16 a[170:171], v4 offset:36864\n"
    "v_mfma_f32_16x16x32_bf16 v[136:139], a[140:143], a[76:79], v[136:139]\n"
    "ds_read_b64_tr_b16 a[172:173], v4 offset:40960\n"
    "ds_read_b64_tr_b16 a[174:175], v4 offset:45056\n"
    "v_mfma_f32_16x16x32_bf16 v[152:155], a[128:131], a[80:83], v[216:219]\n"
    "v_exp_f32 v145, v145\n"
    "v_mfma_f32_16x16x32_bf16 v[152:155], a[132:135], a[84:87], v[152:155]\n"
    "s_waitcnt lgkmcnt(4)\n"
    "buffer_store_dwordx4 v[236:239], v11, s[52:55], s72 offen offset:2176 sc1\n"
    "v_cvt_pk_bf16_f32 v192, v144, v145\n"
    "v_mfma_f32_16x16x32_bf16 v[152:155], a[136:139], a[88:91], v[152:155]\n"
    "v_exp_f32 v146, v146\n"
    "v_mfma_f32_16x16x32_bf16 v[152:155], a[140:143], a[92:95], v[152:155]\n"
    "ds_read_b64_tr_b16 a[176:177], v3 offset:33280\n"
    "ds_read_b64_tr_b16 a[178:179], v3 offset:37376\n"
    "v_mfma_f32_16x16x32_bf16 v[124:127], a[144:147], a[48:51], v[208:211]\n"
    "v_exp_f32 v147, v147\n"
    "v_mfma_f32_16x16x32_bf16 v[124:127], a[148:151], a[52:55], v[124:127]\n"
    "ds_read_b64_tr_b16 a[180:181], v3 offset:41472\n"
    "ds_read_b64_tr_b16 a[182:183], v3 offset:45568\n"
    "v_mfma_f32_16x16x32_bf16 v[124:127], a[152:155], a[56:59], v[124:127]\n"
    "v_exp_f32 v116, v116\n"
    "v_mfma_f32_16x16x32_bf16 v[124:127], a[156:159], a[60:63], v[124:127]\n"
    "ds_read_b64_tr_b16 a[186:187], v4 offset:37376\n"
    "v_cvt_pk_bf16_f32 v193, v146, v147\n"
    "v_mfma_f32_16x16x32_bf16 v[140:143], a[144:147], a[64:67], v[212:215]\n"
    "v_exp_f32 v117, v117\n"
    "v_mfma_f32_16x16x32_bf16 v[140:143], a[148:151], a[68:71], v[140:143]\n"
    "ds_read_b64_tr_b16 a[184:185], v4 offset:33280\n"
    "v_cvt_pk_bf16_f32 v162, v116, v117\n"
    "v_mfma_f32_16x16x32_bf16 v[140:143], a[152:155], a[72:75], v[140:143]\n"
    "v_exp_f32 v118, v118\n"
    "v_mfma_f32_16x16x32_bf16 v[140:143], a[156:159], a[76:79], v[140:143]\n"
    "ds_read_b64_tr_b16 a[188:189], v4 offset:41472\n"
    "ds_read_b64_tr_b16 a[190:191], v4 offset:45568\n"
    "v_mfma_f32_16x16x32_bf16 v[156:159], a[144:147], a[80:83], v[216:219]\n"
    "v_exp_f32 v119, v119\n"
    "v_mfma_f32_16x16x32_bf16 v[156:159], a[148:151], a[84:87], v[156:159]\n"
    "ds_read_b128 a[96:99], v1 offset:16384\n"
    "v_cvt_pk_bf16_f32 v163, v118, v119\n"
    "v_mfma_f32_16x16x32_bf16 v[156:159], a[152:155], a[88:91], v[156:159]\n"
    "v_exp_f32 v132, v132\n"
    "v_mfma_f32_16x16x32_bf16 v[156:159], a[156:159], a[92:95], v[156:159]\n"
    "s_add_u32 s64, s59, 83968\n"
    "ds_read_b64_tr_b16 a[192:193], v3 offset:33792\n"
    "ds_read_b64_tr_b16 a[194:195], v3 offset:37888\n"
    "v_mfma_f32_16x16x32_bf16 a[224:227], a[236:239], v[168:171], a[224:227]\n"
    "s_add_u32 m0, s60, 18432\n"
    "v_exp_f32 v133, v133\n"
    "v_mfma_f32_16x16x32_bf16 a[224:227], a[236:239], v[172:175], a[224:227]\n"
    "buffer_load_dwordx4 v7, s[44:47], s64 offen nt lds\n"
    "v_mfma_f32_16x16x32_bf16 a[228:231], a[236:239], v[184:187], a[228:231]\n"
    "v_exp_f32 v134, v134\n"
    "v_mfma_f32_16x16x32_bf16 a[228:231], a[236:239], v[188:191], a[228:231]\n"
    "ds_read_b64_tr_b16 a[196:197], v3 offset:41984\n"
    "ds_read_b64_tr_b16 a[198:199], v3 offset:46080\n"
    "v_mfma_f32_16x16x32_bf16 a[232:235], a[236:239], v[200:203], a[232:235]\n"
    "v_exp_f32 v135, v135\n"
    "v_mfma_f32_16x16x32_bf16 a[232:235], a[236:239], v[204:207], a[232:235]\n"
    "ds_read_b128 a[100:103], v2 offset:16384\n"
    "v_cvt_pk_bf16_f32 v178, v132, v133\n"
    "v_mfma_f32_16x16x32_bf16 v[16:19], a[160:163], v[168:171], v[16:19]\n"
    "v_exp_f32 v148, v148\n"
    "v_mfma_f32_16x16x32_bf16 v[16:19], a[164:167], v[172:175], v[16:19]\n"
    "ds_read_b128 a[104:107], v1 offset:24576\n"
    "v_cvt_pk_bf16_f32 v179, v134, v135\n"
    "v_mfma_f32_16x16x32_bf16 v[20:23], a[160:163], v[184:187], v[20:23]\n"
    "ds_read_b64_tr_b16 a[200:201], v4 offset:33792\n"
    "ds_read_b64_tr_b16 a[202:203], v4 offset:37888\n"
    "v_mfma_f32_16x16x32_bf16 v[20:23], a[164:167], v[188:191], v[20:23]\n"
    "v_exp_f32 v149, v149\n"
    "v_mfma_f32_16x16x32_bf16 v[24:27], a[160:163], v[200:203], v[24:27]\n"
    "ds_read_b128 a[108:111], v2 offset:24576\n"
    "v_cvt_pk_bf16_f32 v194, v148, v149\n"
    "v_mfma_f32_16x16x32_bf16 v[24:27], a[164:167], v[204:207], v[24:27]\n"
    "s_waitcnt lgkmcnt(5)\n"
    "v_exp_f32 v150, v150\n"
    "v_mfma_f32_16x16x32_bf16 v[28:31], a[168:171], v[168:171], v[28:31]\n"
    "ds_read_b64_tr_b16 a[204:205], v4 offset:41984\n"
    "ds_read_b64_tr_b16 a[206:207], v4 offset:46080\n"
    "v_mfma_f32_16x16x32_bf16 v[28:31], a[172:175], v[172:175], v[28:31]\n"
    "v_exp_f32 v151, v151\n"
    "v_mfma_f32_16x16x32_bf16 v[32:35], a[168:171], v[184:187], v[32:35]\n"
    "ds_read_b128 a[112:115], v1 offset:18432\n"
    "v_cvt_pk_bf16_f32 v195, v150, v151\n"
    "v_mfma_f32_16x16x32_bf16 v[32:35], a[172:175], v[188:191], v[32:35]\n"
    "v_exp_f32 v120, v120\n"
    "v_mfma_f32_16x16x32_bf16 v[36:39], a[168:171], v[200:203], v[36:39]\n"
    "ds_read_b128 a[116:119], v2 offset:18432\n"
    "v_mfma_f32_16x16x32_bf16 v[36:39], a[172:175], v[204:207], v[36:39]\n"
    "v_exp_f32 v121, v121\n"
    "v_mfma_f32_16x16x32_bf16 v[40:43], a[176:179], v[168:171], v[40:43]\n"
    "ds_read_b64_tr_b16 a[208:209], v3 offset:34304\n"
    "ds_read_b64_tr_b16 a[210:211], v3 offset:38400\n"
    "v_mfma_f32_16x16x32_bf16 v[40:43], a[180:183], v[172:175], v[40:43]\n"
    "v_exp_f32 v122, v122\n"
    "v_mfma_f32_16x16x32_bf16 v[44:47], a[176:179], v[184:187], v[44:47]\n"
    "ds_read_b128 a[120:123], v1 offset:26624\n"
    "v_cvt_pk_bf16_f32 v164, v120, v121\n"
    "v_mfma_f32_16x16x32_bf16 v[44:47], a[180:183], v[188:191], v[44:47]\n"
    "v_exp_f32 v123, v123\n"
    "v_mfma_f32_16x16x32_bf16 v[48:51], a[176:179], v[200:203], v[48:51]\n"
    "s_add_u32 s65, s59, 84096\n"
    "ds_read_b64_tr_b16 a[212:213], v3 offset:42496\n"
    "ds_read_b64_tr_b16 a[214:215], v3 offset:46592\n"
    "v_mfma_f32_16x16x32_bf16 v[48:51], a[180:183], v[204:207], v[48:51]\n"
    "s_add_u32 m0, s60, 19456\n"
    "v_exp_f32 v136, v136\n"
    "v_mfma_f32_16x16x32_bf16 v[52:55], a[184:187], v[168:171], v[52:55]\n"
    "buffer_load_dwordx4 v7, s[44:47], s65 offen nt lds\n"
    "v_mfma_f32_16x16x32_bf16 v[52:55], a[188:191], v[172:175], v[52:55]\n"
    "ds_read_b128 a[156:159], v2 offset:30720\n"
    "v_exp_f32 v137, v137\n"
    "v_mfma_f32_16x16x32_bf16 v[56:59], a[184:187], v[184:187], v[56:59]\n"
    "ds_read_b128 a[124:127], v2 offset:26624\n"
    "v_cvt_pk_bf16_f32 v165, v122, v123\n"
    "v_mfma_f32_16x16x32_bf16 v[56:59], a[188:191], v[188:191], v[56:59]\n"
    "v_exp_f32 v138, v138\n"
    "v_mfma_f32_16x16x32_bf16 v[60:63], a[184:187], v[200:203], v[60:63]\n"
    "ds_read_b128 a[128:131], v1 offset:20480\n"
    "v_cvt_pk_bf16_f32 v180, v136, v137\n"
    "v_mfma_f32_16x16x32_bf16 v[60:63], a[188:191], v[204:207], v[60:63]\n"
    "v_exp_f32 v139, v139\n"
    "v_mfma_f32_16x16x32_bf16 v[64:67], a[192:195], v[168:171], v[64:67]\n"
    "ds_read_b64_tr_b16 a[216:217], v4 offset:34304\n"
    "ds_read_b64_tr_b16 a[218:219], v4 offset:38400\n"
    "v_mfma_f32_16x16x32_bf16 v[64:67], a[196:199], v[172:175], v[64:67]\n"
    "ds_read_b64_tr_b16 a[220:221], v4 offset:42496\n"
    "ds_read_b64_tr_b16 a[222:223], v4 offset:46592\n"
    "v_mfma_f32_16x16x32_bf16 v[68:71], a[192:195], v[184:187], v[68:71]\n"
    "v_exp_f32 v152, v152\n"
    "v_mfma_f32_16x16x32_bf16 v[68:71], a[196:199], v[188:191], v[68:71]\n"
    "ds_read_b128 a[132:135], v2 offset:20480\n"
    "v_cvt_pk_bf16_f32 v181, v138, v139\n"
    "v_mfma_f32_16x16x32_bf16 v[72:75], a[192:195], v[200:203], v[72:75]\n"
    "v_exp_f32 v153, v153\n"
    "v_mfma_f32_16x16x32_bf16 v[72:75], a[196:199], v[204:207], v[72:75]\n"
    "s_waitcnt lgkmcnt(5)\n"
    "ds_read_b128 a[136:139], v1 offset:28672\n"
    "v_cvt_pk_bf16_f32 v196, v152, v153\n"
    "v_mfma_f32_16x16x32_bf16 v[76:79], a[200:203], v[168:171], v[76:79]\n"
    "v_exp_f32 v154, v154\n"
    "v_mfma_f32_16x16x32_bf16 v[76:79], a[204:207], v[172:175], v[76:79]\n"
    "ds_read_b128 a[140:143], v2 offset:28672\n"
    "v_mfma_f32_16x16x32_bf16 v[80:83], a[200:203], v[184:187], v[80:83]\n"
    "v_exp_f32 v155, v155\n"
    "v_mfma_f32_16x16x32_bf16 v[80:83], a[204:207], v[188:191], v[80:83]\n"
    "ds_read_b128 a[144:147], v1 offset:22528\n"
    "v_cvt_pk_bf16_f32 v197, v154, v155\n"
    "v_mfma_f32_16x16x32_bf16 v[84:87], a[200:203], v[200:203], v[84:87]\n"
    "v_exp_f32 v124, v124\n"
    "v_mfma_f32_16x16x32_bf16 v[84:87], a[204:207], v[204:207], v[84:87]\n"
    "ds_read_b128 a[148:151], v2 offset:22528\n"
    "v_mfma_f32_16x16x32_bf16 v[88:91], a[208:211], v[168:171], v[88:91]\n"
    "v_exp_f32 v125, v125\n"
    "v_mfma_f32_16x16x32_bf16 v[88:91], a[212:215], v[172:175], v[88:91]\n"
    "ds_read_b128 a[152:155], v1 offset:30720\n"
    "v_cvt_pk_bf16_f32 v166, v124, v125\n"
    "v_mfma_f32_16x16x32_bf16 v[92:95], a[208:211], v[184:187], v[92:95]\n"
    "v_exp_f32 v126, v126\n"
    "v_mfma_f32_16x16x32_bf16 v[92:95], a[212:215], v[188:191], v[92:95]\n"
    "ds_read_b128 v[240:243], v10 offset:6144\n"
    "v_mfma_f32_16x16x32_bf16 v[96:99], a[208:211], v[200:203], v[96:99]\n"
    "v_exp_f32 v127, v127\n"
    "v_mfma_f32_16x16x32_bf16 v[96:99], a[212:215], v[204:207], v[96:99]\n"
    "s_waitcnt lgkmcnt(3)\n"
    "v_cvt_pk_bf16_f32 v167, v126, v127\n"
    "v_mfma_f32_16x16x32_bf16 v[100:103], a[216:219], v[168:171], v[100:103]\n"
    "v_exp_f32 v140, v140\n"
    "v_mfma_f32_16x16x32_bf16 v[100:103], a[220:223], v[172:175], v[100:103]\n"
    "v_mfma_f32_16x16x32_bf16 v[104:107], a[216:219], v[184:187], v[104:107]\n"
    "v_exp_f32 v141, v141\n"
    "v_mfma_f32_16x16x32_bf16 v[104:107], a[220:223], v[188:191], v[104:107]\n"
    "v_cvt_pk_bf16_f32 v182, v140, v141\n"
    "v_mfma_f32_16x16x32_bf16 v[108:111], a[216:219], v[200:203], v[108:111]\n"
    "s_waitcnt vmcnt(11) lgkmcnt(0)\n"
    "v_exp_f32 v142, v142\n"
    "v_mfma_f32_16x16x32_bf16 v[108:111], a[220:223], v[204:207], v[108:111]\n"
    "s_barrier\n"
    "buffer_store_dwordx4 v[240:243], v11, s[52:55], s73 offen offset:128 sc1\n"
    "v_mfma_f32_16x16x32_bf16 v[112:115], a[96:99], a[48:51], v[208:211]\n"
    "s_add_u32 s62, s59, 98304\n"
    "v_exp_f32 v143, v143\n"
    "v_mfma_f32_16x16x32_bf16 v[112:115], a[100:103], a[52:55], v[112:115]\n"
    "s_add_u32 m0, s60, 32768\n"
    "v_cvt_pk_bf16_f32 v183, v142, v143\n"
    "v_mfma_f32_16x16x32_bf16 v[112:115], a[104:107], a[56:59], v[112:115]\n"
    "buffer_load_dwordx4 v7, s[44:47], s62 offen nt lds\n"
    "v_mfma_f32_16x16x32_bf16 v[112:115], a[108:111], a[60:63], v[112:115]\n"
    "v_exp_f32 v156, v156\n"
    "v_mfma_f32_16x16x32_bf16 v[128:131], a[96:99], a[64:67], v[212:215]\n"
    "v_mfma_f32_16x16x32_bf16 v[128:131], a[100:103], a[68:71], v[128:131]\n"
    "v_exp_f32 v157, v157\n"
    "v_mfma_f32_16x16x32_bf16 v[128:131], a[104:107], a[72:75], v[128:131]\n"
    "ds_read_b128 v[220:223], v10 offset:7168\n"
    "v_cvt_pk_bf16_f32 v198, v156, v157\n"
    "v_mfma_f32_16x16x32_bf16 v[128:131], a[108:111], a[76:79], v[128:131]\n"
    "v_exp_f32 v158, v158\n"
    "v_mfma_f32_16x16x32_bf16 v[144:147], a[96:99], a[80:83], v[216:219]\n"
    "v_mfma_f32_16x16x32_bf16 v[144:147], a[100:103], a[84:87], v[144:147]\n"
    "v_exp_f32 v159, v159\n"
    "v_mfma_f32_16x16x32_bf16 v[144:147], a[104:107], a[88:91], v[144:147]\n"
    "v_cvt_pk_bf16_f32 v199, v158, v159\n"
    "v_mfma_f32_16x16x32_bf16 v[144:147], a[108:111], a[92:95], v[144:147]\n"
    "v_exp_f32 v112, v112\n"
    "v_mfma_f32_16x16x32_bf16 v[116:119], a[112:115], a[48:51], v[208:211]\n"
    "s_waitcnt lgkmcnt(0)\n"
    "buffer_store_dwordx4 v[220:223], v11, s[52:55], s73 offen offset:2176 sc1\n"
    "v_mfma_f32_16x16x32_bf16 v[116:119], a[116:119], a[52:55], v[116:119]\n"
    "v_exp_f32 v113, v113\n"
    "v_mfma_f32_16x16x32_bf16 v[116:119], a[120:123], a[56:59], v[116:119]\n"
    "v_cvt_pk_bf16_f32 v168, v112, v113\n"
    "v_mfma_f32_16x16x32_bf16 v[116:119], a[124:127], a[60:63], v[116:119]\n"
    "v_exp_f32 v114, v114\n"
    "v_mfma_f32_16x16x32_bf16 v[132:135], a[112:115], a[64:67], v[212:215]\n"
    "v_mfma_f32_16x16x32_bf16 v[132:135], a[116:119], a[68:71], v[132:135]\n"
    "v_exp_f32 v115, v115\n"
    "v_mfma_f32_16x16x32_bf16 v[132:135], a[120:123], a[72:75], v[132:135]\n"
    "v_cvt_pk_bf16_f32 v169, v114, v115\n"
    "v_mfma_f32_16x16x32_bf16 v[132:135], a[124:127], a[76:79], v[132:135]\n"
    "v_exp_f32 v128, v128\n"
    "v_mfma_f32_16x16x32_bf16 v[148:151], a[112:115], a[80:83], v[216:219]\n"
    "v_mfma_f32_16x16x32_bf16 v[148:151], a[116:119], a[84:87], v[148:151]\n"
    "v_exp_f32 v129, v129\n"
    "v_mfma_f32_16x16x32_bf16 v[148:151], a[120:123], a[88:91], v[148:151]\n"
    "ds_read_b64_tr_b16 a[160:161], v3 offset:0\n"
    "v_cvt_pk_bf16_f32 v184, v128, v129\n"
    "v_mfma_f32_16x16x32_bf16 v[148:151], a[124:127], a[92:95], v[148:151]\n"
    "v_exp_f32 v130, v130\n"
    "v_mfma_f32_16x16x32_bf16 v[120:123], a[128:131], a[48:51], v[208:211]\n"
    "s_add_u32 s63, s59, 98432\n"
    "ds_read_b64_tr_b16 a[162:163], v3 offset:4096\n"
    "ds_read_b64_tr_b16 a[164:165], v3 offset:8192\n"
    "v_mfma_f32_16x16x32_bf16 v[120:123], a[132:135], a[52:55], v[120:123]\n"
    "s_add_u32 m0, s60, 33792\n"
    "v_exp_f32 v131, v131\n"
    "v_mfma_f32_16x16x32_bf16 v[120:123], a[136:139], a[56:59], v[120:123]\n"
    "buffer_load_dwordx4 v7, s[44:47], s63 offen nt lds\n"
    "v_mfma_f32_16x16x32_bf16 v[120:123], a[140:143], a[60:63], v[120:123]\n"
    "ds_read_b64_tr_b16 a[166:167], v3 offset:12288\n"
    "v_cvt_pk_bf16_f32 v185, v130, v131\n"
    "v_mfma_f32_16x16x32_bf16 v[136:139], a[128:131], a[64:67], v[212:215]\n"
    "v_exp_f32 v144, v144\n"
    "v_mfma_f32_16x16x32_bf16 v[136:139], a[132:135], a[68:71], v[136:139]\n"
    "ds_read_b64_tr_b16 a[168:169], v4 offset:0\n"
    "ds_read_b64_tr_b16 a[170:171], v4 offset:4096\n"
    "v_mfma_f32_16x16x32_bf16 v[136:139], a[136:139], a[72:75], v[136:139]\n"
    "v_exp_f32 v145, v145\n"
    "v_mfma_f32_16x16x32_bf16 v[136:139], a[140:143], a[76:79], v[136:139]\n"
    "ds_read_b64_tr_b16 a[172:173], v4 offset:8192\n"
    "ds_read_b64_tr_b16 a[174:175], v4 offset:12288\n"
    "v_mfma_f32_16x16x32_bf16 v[152:155], a[128:131], a[80:83], v[216:219]\n"
    "v_exp_f32 v146, v146\n"
    "v_mfma_f32_16x16x32_bf16 v[152:155], a[132:135], a[84:87], v[152:155]\n"
    "ds_read_b64_tr_b16 a[178:179], v3 offset:4608\n"
    "v_cvt_pk_bf16_f32 v200, v144, v145\n"
    "v_mfma_f32_16x16x32_bf16 v[152:155], a[136:139], a[88:91], v[152:155]\n"
    "v_exp_f32 v147, v147\n"
    "v_mfma_f32_16x16x32_bf16 v[152:155], a[140:143], a[92:95], v[152:155]\n"
    "ds_read_b64_tr_b16 a[176:177], v3 offset:512\n"
    "v_cvt_pk_bf16_f32 v201, v146, v147\n"
    "v_mfma_f32_16x16x32_bf16 v[124:127], a[144:147], a[48:51], v[208:211]\n"
    "v_exp_f32 v116, v116\n"
    "v_mfma_f32_16x16x32_bf16 v[124:127], a[148:151], a[52:55], v[124:127]\n"
    "ds_read_b64_tr_b16 a[180:181], v3 offset:8704\n"
    "ds_read_b64_tr_b16 a[182:183], v3 offset:12800\n"
    "v_mfma_f32_16x16x32_bf16 v[124:127], a[152:155], a[56:59], v[124:127]\n"
    "v_exp_f32 v117, v117\n"
    "v_mfma_f32_16x16x32_bf16 v[124:127], a[156:159], a[60:63], v[124:127]\n"
    "v_cvt_pk_bf16_f32 v170, v116, v117\n"
    "v_mfma_f32_16x16x32_bf16 v[140:143], a[144:147], a[64:67], v[212:215]\n"
    "v_exp_f32 v118, v118\n"
    "v_mfma_f32_16x16x32_bf16 v[140:143], a[148:151], a[68:71], v[140:143]\n"
    "ds_read_b64_tr_b16 a[184:185], v4 offset:512\n"
    "ds_read_b64_tr_b16 a[186:187], v4 offset:4608\n"
    "v_mfma_f32_16x16x32_bf16 v[140:143], a[152:155], a[72:75], v[140:143]\n"
    "v_exp_f32 v119, v119\n"
    "v_mfma_f32_16x16x32_bf16 v[140:143], a[156:159], a[76:79], v[140:143]\n"
    "ds_read_b64_tr_b16 a[188:189], v4 offset:8704\n"
    "ds_read_b64_tr_b16 a[190:191], v4 offset:12800\n"
    "v_mfma_f32_16x16x32_bf16 v[156:159], a[144:147], a[80:83], v[216:219]\n"
    "v_exp_f32 v132, v132\n"
    "v_mfma_f32_16x16x32_bf16 v[156:159], a[148:151], a[84:87], v[156:159]\n"
    "ds_read_b128 a[96:99], v1 offset:32768\n"
    "v_cvt_pk_bf16_f32 v171, v118, v119\n"
    "v_mfma_f32_16x16x32_bf16 v[156:159], a[152:155], a[88:91], v[156:159]\n"
    "v_exp_f32 v133, v133\n"
    "v_mfma_f32_16x16x32_bf16 v[156:159], a[156:159], a[92:95], v[156:159]\n"
    "s_add_u32 s64, s59, 100352\n"
    "ds_read_b64_tr_b16 a[192:193], v3 offset:1024\n"
    "ds_read_b64_tr_b16 a[194:195], v3 offset:5120\n"
    "v_mfma_f32_16x16x32_bf16 a[224:227], a[236:239], v[160:163], a[224:227]\n"
    "s_add_u32 m0, s60, 34816\n"
    "v_exp_f32 v134, v134\n"
    "v_mfma_f32_16x16x32_bf16 a[224:227], a[236:239], v[164:167], a[224:227]\n"
    "buffer_load_dwordx4 v7, s[44:47], s64 offen nt lds\n"
    "v_mfma_f32_16x16x32_bf16 a[228:231], a[236:239], v[176:179], a[228:231]\n"
    "v_exp_f32 v135, v135\n"
    "v_mfma_f32_16x16x32_bf16 a[228:231], a[236:239], v[180:183], a[228:231]\n"
    "ds_read_b64_tr_b16 a[196:197], v3 offset:9216\n"
    "v_cvt_pk_bf16_f32 v186, v132, v133\n"
    "v_mfma_f32_16x16x32_bf16 a[232:235], a[236:239], v[192:195], a[232:235]\n"
    "ds_read_b64_tr_b16 a[198:199], v3 offset:13312\n"
    "v_cvt_pk_bf16_f32 v187, v134, v135\n"
    "v_mfma_f32_16x16x32_bf16 a[232:235], a[236:239], v[196:199], a[232:235]\n"
    "s_waitcnt lgkmcnt(4)\n"
    "v_exp_f32 v148, v148\n"
    "v_mfma_f32_16x16x32_bf16 v[16:19], a[160:163], v[160:163], v[16:19]\n"
    "ds_read_b128 a[100:103], v2 offset:32768\n"
    "v_mfma_f32_16x16x32_bf16 v[16:19], a[164:167], v[164:167], v[16:19]\n"
    "v_exp_f32 v149, v149\n"
    "v_mfma_f32_16x16x32_bf16 v[20:23], a[160:163], v[176:179], v[20:23]\n"
    "ds_read_b128 a[104:107], v1 offset:40960\n"
    "v_cvt_pk_bf16_f32 v202, v148, v149\n"
    "v_mfma_f32_16x16x32_bf16 v[20:23], a[164:167], v[180:183], v[20:23]\n"
    "v_exp_f32 v150, v150\n"
    "v_mfma_f32_16x16x32_bf16 v[24:27], a[160:163], v[192:195], v[24:27]\n"
    "ds_read_b64_tr_b16 a[200:201], v4 offset:1024\n"
    "ds_read_b64_tr_b16 a[202:203], v4 offset:5120\n"
    "v_mfma_f32_16x16x32_bf16 v[24:27], a[164:167], v[196:199], v[24:27]\n"
    "v_exp_f32 v151, v151\n"
    "v_mfma_f32_16x16x32_bf16 v[28:31], a[168:171], v[160:163], v[28:31]\n"
    "ds_read_b128 a[108:111], v2 offset:40960\n"
    "v_cvt_pk_bf16_f32 v203, v150, v151\n"
    "v_mfma_f32_16x16x32_bf16 v[28:31], a[172:175], v[164:167], v[28:31]\n"
    "v_exp_f32 v120, v120\n"
    "v_mfma_f32_16x16x32_bf16 v[32:35], a[168:171], v[176:179], v[32:35]\n"
    "ds_read_b64_tr_b16 a[204:205], v4 offset:9216\n"
    "ds_read_b64_tr_b16 a[206:207], v4 offset:13312\n"
    "v_mfma_f32_16x16x32_bf16 v[32:35], a[172:175], v[180:183], v[32:35]\n"
    "v_exp_f32 v121, v121\n"
    "v_mfma_f32_16x16x32_bf16 v[36:39], a[168:171], v[192:195], v[36:39]\n"
    "ds_read_b128 a[112:115], v1 offset:34816\n"
    "v_cvt_pk_bf16_f32 v172, v120, v121\n"
    "v_mfma_f32_16x16x32_bf16 v[36:39], a[172:175], v[196:199], v[36:39]\n"
    "v_exp_f32 v122, v122\n"
    "v_mfma_f32_16x16x32_bf16 v[40:43], a[176:179], v[160:163], v[40:43]\n"
    "ds_read_b128 a[116:119], v2 offset:34816\n"
    "v_mfma_f32_16x16x32_bf16 v[40:43], a[180:183], v[164:167], v[40:43]\n"
    "v_exp_f32 v123, v123\n"
    "v_mfma_f32_16x16x32_bf16 v[44:47], a[176:179], v[176:179], v[44:47]\n"
    "ds_read_b64_tr_b16 a[208:209], v3 offset:1536\n"
    "ds_read_b64_tr_b16 a[210:211], v3 offset:5632\n"
    "v_mfma_f32_16x16x32_bf16 v[44:47], a[180:183], v[180:183], v[44:47]\n"
    "v_exp_f32 v136, v136\n"
    "v_mfma_f32_16x16x32_bf16 v[48:51], a[176:179], v[192:195], v[48:51]\n"
    "s_add_u32 s65, s59, 100480\n"
    "ds_read_b128 a[120:123], v1 offset:43008\n"
    "v_cvt_pk_bf16_f32 v173, v122, v123\n"
    "v_mfma_f32_16x16x32_bf16 v[48:51], a[180:183], v[196:199], v[48:51]\n"
    "s_add_u32 m0, s60, 35840\n"
    "v_exp_f32 v137, v137\n"
    "v_mfma_f32_16x16x32_bf16 v[52:55], a[184:187], v[160:163], v[52:55]\n"
    "buffer_load_dwordx4 v7, s[44:47], s65 offen nt lds\n"
    "ds_read_b128 a[156:159], v2 offset:47104\n"
    "v_mfma_f32_16x16x32_bf16 v[52:55], a[188:191], v[164:167], v[52:55]\n"
    "ds_read_b128 a[152:155], v1 offset:47104\n"
    "v_exp_f32 v138, v138\n"
    "v_mfma_f32_16x16x32_bf16 v[56:59], a[184:187], v[176:179], v[56:59]\n"
    "ds_read_b64_tr_b16 a[212:213], v3 offset:9728\n"
    "ds_read_b64_tr_b16 a[214:215], v3 offset:13824\n"
    "v_mfma_f32_16x16x32_bf16 v[56:59], a[188:191], v[180:183], v[56:59]\n"
    "v_exp_f32 v139, v139\n"
    "v_mfma_f32_16x16x32_bf16 v[60:63], a[184:187], v[192:195], v[60:63]\n"
    "ds_read_b128 a[124:127], v2 offset:43008\n"
    "v_cvt_pk_bf16_f32 v188, v136, v137\n"
    "v_mfma_f32_16x16x32_bf16 v[60:63], a[188:191], v[196:199], v[60:63]\n"
    "s_waitcnt lgkmcnt(5)\n"
    "ds_read_b64_tr_b16 a[216:217], v4 offset:1536\n"
    "ds_read_b64_tr_b16 a[218:219], v4 offset:5632\n"
    "v_mfma_f32_16x16x32_bf16 v[64:67], a[192:195], v[160:163], v[64:67]\n"
    "v_exp_f32 v152, v152\n"
    "v_mfma_f32_16x16x32_bf16 v[64:67], a[196:199], v[164:167], v[64:67]\n"
    "ds_read_b128 a[128:131], v1 offset:36864\n"
    "v_cvt_pk_bf16_f32 v189, v138, v139\n"
    "v_mfma_f32_16x16x32_bf16 v[68:71], a[192:195], v[176:179], v[68:71]\n"
    "v_exp_f32 v153, v153\n"
    "v_mfma_f32_16x16x32_bf16 v[68:71], a[196:199], v[180:183], v[68:71]\n"
    "ds_read_b128 a[132:135], v2 offset:36864\n"
    "v_cvt_pk_bf16_f32 v204, v152, v153\n"
    "v_mfma_f32_16x16x32_bf16 v[72:75], a[192:195], v[192:195], v[72:75]\n"
    "v_exp_f32 v154, v154\n"
    "v_mfma_f32_16x16x32_bf16 v[72:75], a[196:199], v[196:199], v[72:75]\n"
    "ds_read_b64_tr_b16 a[220:221], v4 offset:9728\n"
    "ds_read_b64_tr_b16 a[222:223], v4 offset:13824\n"
    "v_mfma_f32_16x16x32_bf16 v[76:79], a[200:203], v[160:163], v[76:79]\n"
    "v_exp_f32 v155, v155\n"
    "v_mfma_f32_16x16x32_bf16 v[76:79], a[204:207], v[164:167], v[76:79]\n"
    "ds_read_b128 a[136:139], v1 offset:45056\n"
    "v_cvt_pk_bf16_f32 v205, v154, v155\n"
    "v_mfma_f32_16x16x32_bf16 v[80:83], a[200:203], v[176:179], v[80:83]\n"
    "v_exp_f32 v124, v124\n"
    "v_mfma_f32_16x16x32_bf16 v[80:83], a[204:207], v[180:183], v[80:83]\n"
    "ds_read_b128 a[140:143], v2 offset:45056\n"
    "v_mfma_f32_16x16x32_bf16 v[84:87], a[200:203], v[192:195], v[84:87]\n"
    "v_exp_f32 v125, v125\n"
    "v_mfma_f32_16x16x32_bf16 v[84:87], a[204:207], v[196:199], v[84:87]\n"
    "ds_read_b128 a[144:147], v1 offset:38912\n"
    "v_cvt_pk_bf16_f32 v174, v124, v125\n"
    "v_mfma_f32_16x16x32_bf16 v[88:91], a[208:211], v[160:163], v[88:91]\n"
    "s_waitcnt lgkmcnt(3)\n"
    "v_exp_f32 v126, v126\n"
    "v_mfma_f32_16x16x32_bf16 v[88:91], a[212:215], v[164:167], v[88:91]\n"
    "ds_read_b128 a[148:151], v2 offset:38912\n"
    "v_mfma_f32_16x16x32_bf16 v[92:95], a[208:211], v[176:179], v[92:95]\n"
    "v_exp_f32 v127, v127\n"
    "v_mfma_f32_16x16x32_bf16 v[92:95], a[212:215], v[180:183], v[92:95]\n"
    "ds_read_b128 v[224:227], v10 offset:10240\n"
    "v_cvt_pk_bf16_f32 v175, v126, v127\n"
    "v_mfma_f32_16x16x32_bf16 v[96:99], a[208:211], v[192:195], v[96:99]\n"
    "v_exp_f32 v140, v140\n"
    "v_mfma_f32_16x16x32_bf16 v[96:99], a[212:215], v[196:199], v[96:99]\n"
    "ds_read_b128 v[228:231], v10 offset:11264\n"
    "v_mfma_f32_16x16x32_bf16 v[100:103], a[216:219], v[160:163], v[100:103]\n"
    "v_exp_f32 v141, v141\n"
    "v_mfma_f32_16x16x32_bf16 v[100:103], a[220:223], v[164:167], v[100:103]\n"
    "v_cvt_pk_bf16_f32 v190, v140, v141\n"
    "v_mfma_f32_16x16x32_bf16 v[104:107], a[216:219], v[176:179], v[104:107]\n"
    "v_exp_f32 v142, v142\n"
    "v_mfma_f32_16x16x32_bf16 v[104:107], a[220:223], v[180:183], v[104:107]\n"
    "s_waitcnt lgkmcnt(1)\n"
    "buffer_store_dwordx4 v[224:227], v11, s[52:55], s74 offen offset:128 sc1\n"
    "v_mfma_f32_16x16x32_bf16 v[108:111], a[216:219], v[192:195], v[108:111]\n"
    "s_waitcnt vmcnt(7) lgkmcnt(0)\n"
    "v_mfma_f32_16x16x32_bf16 v[108:111], a[220:223], v[196:199], v[108:111]\n"
    "s_barrier\n"
    "v_exp_f32 v143, v143\n"
    "v_mfma_f32_16x16x32_bf16 v[112:115], a[96:99], a[48:51], v[208:211]\n"
    "s_add_u32 s62, s59, 114688\n"
    "v_cvt_pk_bf16_f32 v191, v142, v143\n"
    "v_mfma_f32_16x16x32_bf16 v[112:115], a[100:103], a[52:55], v[112:115]\n"
    "s_add_u32 m0, s60, 0\n"
    "v_exp_f32 v156, v156\n"
    "v_mfma_f32_16x16x32_bf16 v[112:115], a[104:107], a[56:59], v[112:115]\n"
    "buffer_load_dwordx4 v7, s[44:47], s62 offen nt lds\n"
    "v_mfma_f32_16x16x32_bf16 v[112:115], a[108:111], a[60:63], v[112:115]\n"
    "v_exp_f32 v157, v157\n"
    "v_mfma_f32_16x16x32_bf16 v[128:131], a[96:99], a[64:67], v[212:215]\n"
    "v_cvt_pk_bf16_f32 v206, v156, v157\n"
    "v_mfma_f32_16x16x32_bf16 v[128:131], a[100:103], a[68:71], v[128:131]\n"
    "v_exp_f32 v158, v158\n"
    "v_mfma_f32_16x16x32_bf16 v[128:131], a[104:107], a[72:75], v[128:131]\n"
    "buffer_store_dwordx4 v[228:231], v11, s[52:55], s74 offen offset:2176 sc1\n"
    "v_mfma_f32_16x16x32_bf16 v[128:131], a[108:111], a[76:79], v[128:131]\n"
    "v_exp_f32 v159, v159\n"
    "v_mfma_f32_16x16x32_bf16 v[144:147], a[96:99], a[80:83], v[216:219]\n"
    "v_cvt_pk_bf16_f32 v207, v158, v159\n"
    "v_mfma_f32_16x16x32_bf16 v[144:147], a[100:103], a[84:87], v[144:147]\n"
    "v_exp_f32 v112, v112\n"
    "v_mfma_f32_16x16x32_bf16 v[144:147], a[104:107], a[88:91], v[144:147]\n"
    "v_mfma_f32_16x16x32_bf16 v[144:147], a[108:111], a[92:95], v[144:147]\n"
    "v_exp_f32 v113, v113\n"
    "v_mfma_f32_16x16x32_bf16 v[116:119], a[112:115], a[48:51], v[208:211]\n"
    "v_cvt_pk_bf16_f32 v160, v112, v113\n"
    "v_mfma_f32_16x16x32_bf16 v[116:119], a[116:119], a[52:55], v[116:119]\n"
    "v_exp_f32 v114, v114\n"
    "v_mfma_f32_16x16x32_bf16 v[116:119], a[120:123], a[56:59], v[116:119]\n"
    "v_mfma_f32_16x16x32_bf16 v[116:119], a[124:127], a[60:63], v[116:119]\n"
    "v_exp_f32 v115, v115\n"
    "v_mfma_f32_16x16x32_bf16 v[132:135], a[112:115], a[64:67], v[212:215]\n"
    "v_cvt_pk_bf16_f32 v161, v114, v115\n"
    "v_mfma_f32_16x16x32_bf16 v[132:135], a[116:119], a[68:71], v[132:135]\n"
    "v_exp_f32 v128, v128\n"
    "v_mfma_f32_16x16x32_bf16 v[132:135], a[120:123], a[72:75], v[132:135]\n"
    "v_mfma_f32_16x16x32_bf16 v[132:135], a[124:127], a[76:79], v[132:135]\n"
    "v_exp_f32 v129, v129\n"
    "v_mfma_f32_16x16x32_bf16 v[148:151], a[112:115], a[80:83], v[216:219]\n"
    "v_cvt_pk_bf16_f32 v176, v128, v129\n"
    "v_mfma_f32_16x16x32_bf16 v[148:151], a[116:119], a[84:87], v[148:151]\n"
    "v_exp_f32 v130, v130\n"
    "v_mfma_f32_16x16x32_bf16 v[148:151], a[120:123], a[88:91], v[148:151]\n"
    "ds_read_b64_tr_b16 a[164:165], v3 offset:24576\n"
    "ds_read_b64_tr_b16 a[166:167], v3 offset:28672\n"
    "v_mfma_f32_16x16x32_bf16 v[148:151], a[124:127], a[92:95], v[148:151]\n"
    "s_add_u32 s63, s59, 114816\n"
    "ds_read_b64_tr_b16 a[160:161], v3 offset:16384\n"
    "ds_read_b64_tr_b16 a[162:163], v3 offset:20480\n"
    "v_mfma_f32_16x16x32_bf16 v[120:123], a[128:131], a[48:51], v[208:211]\n"
    "s_add_u32 m0, s60, 1024\n"
    "v_exp_f32 v131, v131\n"
    "v_mfma_f32_16x16x32_bf16 v[120:123], a[132:135], a[52:55], v[120:123]\n"
    "buffer_load_dwordx4 v7, s[44:47], s63 offen nt lds\n"
    "v_mfma_f32_16x16x32_bf16 v[120:123], a[136:139], a[56:59], v[120:123]\n"
    "v_exp_f32 v144, v144\n"
    "v_mfma_f32_16x16x32_bf16 v[120:123], a[140:143], a[60:63], v[120:123]\n"
    "ds_read_b64_tr_b16 a[170:171], v4 offset:20480\n"
    "v_cvt_pk_bf16_f32 v177, v130, v131\n"
    "v_mfma_f32_16x16x32_bf16 v[136:139], a[128:131], a[64:67], v[212:215]\n"
    "v_exp_f32 v145, v145\n"
    "v_mfma_f32_16x16x32_bf16 v[136:139], a[132:135], a[68:71], v[136:139]\n"
    "ds_read_b64_tr_b16 a[168:169], v4 offset:16384\n"
    "v_cvt_pk_bf16_f32 v192, v144, v145\n"
    "v_mfma_f32_16x16x32_bf16 v[136:139], a[136:139], a[72:75], v[136:139]\n"
    "v_exp_f32 v146, v146\n"
    "v_mfma_f32_16x16x32_bf16 v[136:139], a[140:143], a[76:79], v[136:139]\n"
    "ds_read_b64_tr_b16 a[172:173], v4 offset:24576\n"
    "ds_read_b64_tr_b16 a[174:175], v4 offset:28672\n"
    "v_mfma_f32_16x16x32_bf16 v[152:155], a[128:131], a[80:83], v[216:219]\n"
    "v_exp_f32 v147, v147\n"
    "v_mfma_f32_16x16x32_bf16 v[152:155], a[132:135], a[84:87], v[152:155]\n"
    "v_cvt_pk_bf16_f32 v193, v146, v147\n"
    "v_mfma_f32_16x16x32_bf16 v[152:155], a[136:139], a[88:91], v[152:155]\n"
    "v_exp_f32 v116, v116\n"
    "v_mfma_f32_16x16x32_bf16 v[152:155], a[140:143], a[92:95], v[152:155]\n"
    "ds_read_b64_tr_b16 a[176:177], v3 offset:16896\n"
    "ds_read_b64_tr_b16 a[178:179], v3 offset:20992\n"
    "v_mfma_f32_16x16x32_bf16 v[124:127], a[144:147], a[48:51], v[208:211]\n"
    "v_exp_f32 v117, v117\n"
    "v_mfma_f32_16x16x32_bf16 v[124:127], a[148:151], a[52:55], v[124:127]\n"
    "ds_read_b64_tr_b16 a[180:181], v3 offset:25088\n"
    "ds_read_b64_tr_b16 a[182:183], v3 offset:29184\n"
    "v_mfma_f32_16x16x32_bf16 v[124:127], a[152:155], a[56:59], v[124:127]\n"
    "v_exp_f32 v118, v118\n"
    "v_mfma_f32_16x16x32_bf16 v[124:127], a[156:159], a[60:63], v[124:127]\n"
    "ds_read_b64_tr_b16 a[190:191], v4 offset:29184\n"
    "v_cvt_pk_bf16_f32 v162, v116, v117\n"
    "v_mfma_f32_16x16x32_bf16 v[140:143], a[144:147], a[64:67], v[212:215]\n"
    "v_exp_f32 v119, v119\n"
    "v_mfma_f32_16x16x32_bf16 v[140:143], a[148:151], a[68:71], v[140:143]\n"
    "ds_read_b64_tr_b16 a[184:185], v4 offset:16896\n"
    "ds_read_b64_tr_b16 a[186:187], v4 offset:20992\n"
    "v_mfma_f32_16x16x32_bf16 v[140:143], a[152:155], a[72:75], v[140:143]\n"
    "Lpg7%=:\n"
    "s_cbranch_scc1 Lback%=\n"
    "v_exp_f32 v132, v132\n"
    "v_mfma_f32_16x16x32_bf16 v[140:143], a[156:159], a[76:79], v[140:143]\n"
    "ds_read_b64_tr_b16 a[188:189], v4 offset:25088\n"
    "v_cvt_pk_bf16_f32 v163, v118, v119\n"
    "v_mfma_f32_16x16x32_bf16 v[156:159], a[144:147], a[80:83], v[216:219]\n"
    "v_exp_f32 v133, v133\n"
    "v_mfma_f32_16x16x32_bf16 v[156:159], a[148:151], a[84:87], v[156:159]\n"
    "ds_read_b128 a[96:99], v1 offset:49152\n"
    "v_cvt_pk_bf16_f32 v178, v132, v133\n"
    "v_mfma_f32_16x16x32_bf16 v[156:159], a[152:155], a[88:91], v[156:159]\n"
    "v_exp_f32 v134, v134\n"
    "v_mfma_f32_16x16x32_bf16 v[156:159], a[156:159], a[92:95], v[156:159]\n"
    "ds_read_b64_tr_b16 a[192:193], v3 offset:17408\n"
    "ds_read_b64_tr_b16 a[194:195], v3 offset:21504\n"
    "v_mfma_f32_16x16x32_bf16 a[224:227], a[236:239], v[168:171], a[224:227]\n"
    "s_add_u32 s64, s59, 116736\n"
    "ds_read_b64_tr_b16 a[196:197], v3 offset:25600\n"
    "ds_read_b64_tr_b16 a[198:199], v3 offset:29696\n"
    "v_mfma_f32_16x16x32_bf16 a[224:227], a[236:239], v[172:175], a[224:227]\n"
    "s_add_u32 m0, s60, 2048\n"
    "v_exp_f32 v135, v135\n"
    "v_mfma_f32_16x16x32_bf16 a[228:231], a[236:239], v[184:187], a[228:231]\n"
    "buffer_load_dwordx4 v7, s[44:47], s64 offen nt lds\n"
    "v_mfma_f32_16x16x32_bf16 a[228:231], a[236:239], v[188:191], a[228:231]\n"
    "v_exp_f32 v148, v148\n"
    "v_mfma_f32_16x16x32_bf16 a[232:235], a[236:239], v[200:203], a[232:235]\n"
    "ds_read_b128 a[100:103], v2 offset:49152\n"
    "v_cvt_pk_bf16_f32 v179, v134, v135\n"
    "v_mfma_f32_16x16x32_bf16 a[232:235], a[236:239], v[204:207], a[232:235]\n"
    "s_waitcnt lgkmcnt(5)\n"
    "v_exp_f32 v149, v149\n"
    "v_mfma_f32_16x16x32_bf16 v[16:19], a[160:163], v[168:171], v[16:19]\n"
    "ds_read_b128 a[104:107], v1 offset:57344\n"
    "v_cvt_pk_bf16_f32 v194, v148, v149\n"
    "v_mfma_f32_16x16x32_bf16 v[16:19], a[164:167], v[172:175], v[16:19]\n"
    "v_exp_f32 v150, v150\n"
    "v_mfma_f32_16x16x32_bf16 v[20:23], a[160:163], v[184:187], v[20:23]\n"
    "ds_read_b64_tr_b16 a[200:201], v4 offset:17408\n"
    "ds_read_b64_tr_b16 a[202:203], v4 offset:21504\n"
    "v_mfma_f32_16x16x32_bf16 v[20:23], a[164:167], v[188:191], v[20:23]\n"
    "v_exp_f32 v151, v151\n"
    "v_mfma_f32_16x16x32_bf16 v[24:27], a[160:163], v[200:203], v[24:27]\n"
    "ds_read_b128 a[108:111], v2 offset:57344\n"
    "v_cvt_pk_bf16_f32 v195, v150, v151\n"
    "v_mfma_f32_16x16x32_bf16 v[24:27], a[164:167], v[204:207], v[24:27]\n"
    "v_exp_f32 v120, v120\n"
    "v_mfma_f32_16x16x32_bf16 v[28:31], a[168:171], v[168:171], v[28:31]\n"
    "ds_read_b64_tr_b16 a[204:205], v4 offset:25600\n"
    "ds_read_b64_tr_b16 a[206:207], v4 offset:29696\n"
    "v_mfma_f32_16x16x32_bf16 v[28:31], a[172:175], v[172:175], v[28:31]\n"
    "v_exp_f32 v121, v121\n"
    "v_mfma_f32_16x16x32_bf16 v[32:35], a[168:171], v[184:187], v[32:35]\n"
    "ds_read_b128 a[112:115], v1 offset:51200\n"
    "v_cvt_pk_bf16_f32 v164, v120, v121\n"
    "v_mfma_f32_16x16x32_bf16 v[32:35], a[172:175], v[188:191], v[32:35]\n"
    "v_exp_f32 v122, v122\n"
    "v_mfma_f32_16x16x32_bf16 v[36:39], a[168:171], v[200:203], v[36:39]\n"
    "ds_read_b128 a[116:119], v2 offset:51200\n"
    "v_mfma_f32_16x16x32_bf16 v[36:39], a[172:175], v[204:207], v[36:39]\n"
    "v_exp_f32 v123, v123\n"
    "v_mfma_f32_16x16x32_bf16 v[40:43], a[176:179], v[168:171], v[40:43]\n"
    "ds_read_b64_tr_b16 a[208:209], v3 offset:17920\n"
    "ds_read_b64_tr_b16 a[210:211], v3 offset:22016\n"
    "v_mfma_f32_16x16x32_bf16 v[40:43], a[180:183], v[172:175], v[40:43]\n"
    "v_exp_f32 v136, v136\n"
    "v_mfma_f32_16x16x32_bf16 v[44:47], a[176:179], v[184:187], v[44:47]\n"
    "ds_read_b128 a[120:123], v1 offset:59392\n"
    "v_cvt_pk_bf16_f32 v165, v122, v123\n"
    "v_mfma_f32_16x16x32_bf16 v[44:47], a[180:183], v[188:191], v[44:47]\n"
    "v_exp_f32 v137, v137\n"
    "v_mfma_f32_16x16x32_bf16 v[48:51], a[176:179], v[200:203], v[48:51]\n"
    "ds_read_b64_tr_b16 a[212:213], v3 offset:26112\n"
    "ds_read_b64_tr_b16 a[214:215], v3 offset:30208\n"
    "v_mfma_f32_16x16x32_bf16 v[48:51], a[180:183], v[204:207], v[48:51]\n"
    "s_add_u32 s65, s59, 116864\n"
    "v_exp_f32 v138, v138\n"
    "v_mfma_f32_16x16x32_bf16 v[52:55], a[184:187], v[168:171], v[52:55]\n"
    "s_add_u32 m0, s60, 3072\n"
    "ds_read_b128 a[124:127], v2 offset:59392\n"
    "v_cvt_pk_bf16_f32 v180, v136, v137\n"
    "v_mfma_f32_16x16x32_bf16 v[52:55], a[188:191], v[172:175], v[52:55]\n"
    "buffer_load_dwordx4 v7, s[44:47], s65 offen nt lds\n"
    "v_mfma_f32_16x16x32_bf16 v[56:59], a[184:187], v[184:187], v[56:59]\n"
    "ds_read_b128 a[156:159], v2 offset:63488\n"
    "v_exp_f32 v139, v139\n"
    "v_mfma_f32_16x16x32_bf16 v[56:59], a[188:191], v[188:191], v[56:59]\n"
    "ds_read_b128 a[128:131], v1 offset:53248\n"
    "v_cvt_pk_bf16_f32 v181, v138, v139\n"
    "v_mfma_f32_16x16x32_bf16 v[60:63], a[184:187], v[200:203], v[60:63]\n"
    "v_exp_f32 v152, v152\n"
    "v_mfma_f32_16x16x32_bf16 v[60:63], a[188:191], v[204:207], v[60:63]\n"
    "s_waitcnt lgkmcnt(3)\n"
    "ds_read_b64_tr_b16 a[216:217], v4 offset:17920\n"
    "ds_read_b64_tr_b16 a[218:219], v4 offset:22016\n"
    "v_mfma_f32_16x16x32_bf16 v[64:67], a[192:195], v[168:171], v[64:67]\n"
    "v_exp_f32 v153, v153\n"
    "v_mfma_f32_16x16x32_bf16 v[64:67], a[196:199], v[172:175], v[64:67]\n"
    "ds_read_b128 a[132:135], v2 offset:53248\n"
    "v_cvt_pk_bf16_f32 v196, v152, v153\n"
    "v_mfma_f32_16x16x32_bf16 v[68:71], a[192:195], v[184:187], v[68:71]\n"
    "v_exp_f32 v154, v154\n"
    "v_mfma_f32_16x16x32_bf16 v[68:71], a[196:199], v[188:191], v[68:71]\n"
    "ds_read_b64_tr_b16 a[220:221], v4 offset:26112\n"
    "ds_read_b64_tr_b16 a[222:223], v4 offset:30208\n"
    "v_mfma_f32_16x16x32_bf16 v[72:75], a[192:195], v[200:203], v[72:75]\n"
    "v_exp_f32 v155, v155\n"
    "v_mfma_f32_16x16x32_bf16 v[72:75], a[196:199], v[204:207], v[72:75]\n"
    "ds_read_b128 a[136:139], v1 offset:61440\n"
    "v_cvt_pk_bf16_f32 v197, v154, v155\n"
    "v_mfma_f32_16x16x32_bf16 v[76:79], a[200:203], v[168:171], v[76:79]\n"
    "v_exp_f32 v124, v124\n"
    "v_mfma_f32_16x16x32_bf16 v[76:79], a[204:207], v[172:175], v[76:79]\n"
    "ds_read_b128 a[140:143], v2 offset:61440\n"
    "v_mfma_f32_16x16x32_bf16 v[80:83], a[200:203], v[184:187], v[80:83]\n"
    "v_exp_f32 v125, v125\n"
    "v_mfma_f32_16x16x32_bf16 v[80:83], a[204:207], v[188:191], v[80:83]\n"
    "ds_read_b128 a[144:147], v1 offset:55296\n"
    "v_cvt_pk_bf16_f32 v166, v124, v125\n"
    "v_mfma_f32_16x16x32_bf16 v[84:87], a[200:203], v[200:203], v[84:87]\n"
    "v_exp_f32 v126, v126\n"
    "v_mfma_f32_16x16x32_bf16 v[84:87], a[204:207], v[204:207], v[84:87]\n"
    "ds_read_b128 a[148:151], v2 offset:55296\n"
    "v_mfma_f32_16x16x32_bf16 v[88:91], a[208:211], v[168:171], v[88:91]\n"
    "v_exp_f32 v127, v127\n"
    "v_mfma_f32_16x16x32_bf16 v[88:91], a[212:215], v[172:175], v[88:91]\n"
    "ds_read_b128 a[152:155], v1 offset:63488\n"
    "v_cvt_pk_bf16_f32 v167, v126, v127\n"
    "v_mfma_f32_16x16x32_bf16 v[92:95], a[208:211], v[184:187], v[92:95]\n"
    "v_exp_f32 v140, v140\n"
    "v_mfma_f32_16x16x32_bf16 v[92:95], a[212:215], v[188:191], v[92:95]\n"
    "v_mfma_f32_16x16x32_bf16 v[96:99], a[208:211], v[200:203], v[96:99]\n"
    "v_exp_f32 v141, v141\n"
    "v_mfma_f32_16x16x32_bf16 v[96:99], a[212:215], v[204:207], v[96:99]\n"
    "s_waitcnt lgkmcnt(2)\n"
    "v_cvt_pk_bf16_f32 v182, v140, v141\n"
    "v_mfma_f32_16x16x32_bf16 v[100:103], a[216:219], v[168:171], v[100:103]\n"
    "v_exp_f32 v142, v142\n"
    "v_mfma_f32_16x16x32_bf16 v[100:103], a[220:223], v[172:175], v[100:103]\n"
    "v_mfma_f32_16x16x32_bf16 v[104:107], a[216:219], v[184:187], v[104:107]\n"
    "v_mfma_f32_16x16x32_bf16 v[104:107], a[220:223], v[188:191], v[104:107]\n"
    "v_exp_f32 v143, v143\n"
    "v_mfma_f32_16x16x32_bf16 v[108:111], a[216:219], v[200:203], v[108:111]\n"
    "s_waitcnt vmcnt(6) lgkmcnt(0)\n"
    "v_cvt_pk_bf16_f32 v183, v142, v143\n"
    "v_mfma_f32_16x16x32_bf16 v[108:111], a[220:223], v[204:207], v[108:111]\n"
    "s_barrier\n"
    "v_exp_f32 v156, v156\n"
    "v_mfma_f32_16x16x32_bf16 v[112:115], a[96:99], a[48:51], v[208:211]\n"
    "v_mfma_f32_16x16x32_bf16 v[112:115], a[100:103], a[52:55], v[112:115]\n"
    "v_exp_f32 v157, v157\n"
    "v_mfma_f32_16x16x32_bf16 v[112:115], a[104:107], a[56:59], v[112:115]\n"
    "v_cvt_pk_bf16_f32 v198, v156, v157\n"
    "v_mfma_f32_16x16x32_bf16 v[112:115], a[108:111], a[60:63], v[112:115]\n"
    "v_exp_f32 v158, v158\n"
    "v_mfma_f32_16x16x32_bf16 v[128:131], a[96:99], a[64:67], v[212:215]\n"
    "v_mfma_f32_16x16x32_bf16 v[128:131], a[100:103], a[68:71], v[128:131]\n"
    "v_exp_f32 v159, v159\n"
    "v_mfma_f32_16x16x32_bf16 v[128:131], a[104:107], a[72:75], v[128:131]\n"
    "v_cvt_pk_bf16_f32 v199, v158, v159\n"
    "v_mfma_f32_16x16x32_bf16 v[128:131], a[108:111], a[76:79], v[128:131]\n"
    "s_nop 0\n"
    "v_exp_f32 v112, v112\n"
    "v_mfma_f32_16x16x32_bf16 v[144:147], a[96:99], a[80:83], v[216:219]\n"
    "v_mfma_f32_16x16x32_bf16 v[144:147], a[100:103], a[84:87], v[144:147]\n"
    "v_exp_f32 v113, v113\n"
    "v_mfma_f32_16x16x32_bf16 v[144:147], a[104:107], a[88:91], v[144:147]\n"
    "v_cvt_pk_bf16_f32 v168, v112, v113\n"
    "v_mfma_f32_16x16x32_bf16 v[144:147], a[108:111], a[92:95], v[144:147]\n"
    "v_exp_f32 v114, v114\n"
    "v_mfma_f32_16x16x32_bf16 v[116:119], a[112:115], a[48:51], v[208:211]\n"
    "v_mfma_f32_16x16x32_bf16 v[116:119], a[116:119], a[52:55], v[116:119]\n"
    "v_exp_f32 v115, v115\n"
    "v_mfma_f32_16x16x32_bf16 v[116:119], a[120:123], a[56:59], v[116:119]\n"
    "v_cvt_pk_bf16_f32 v169, v114, v115\n"
    "v_mfma_f32_16x16x32_bf16 v[116:119], a[124:127], a[60:63], v[116:119]\n"
    "v_exp_f32 v128, v128\n"
    "v_mfma_f32_16x16x32_bf16 v[132:135], a[112:115], a[64:67], v[212:215]\n"
    "v_mfma_f32_16x16x32_bf16 v[132:135], a[116:119], a[68:71], v[132:135]\n"
    "v_exp_f32 v129, v129\n"
    "v_mfma_f32_16x16x32_bf16 v[132:135], a[120:123], a[72:75], v[132:135]\n"
    "v_cvt_pk_bf16_f32 v184, v128, v129\n"
    "v_mfma_f32_16x16x32_bf16 v[132:135], a[124:127], a[76:79], v[132:135]\n"
    "v_exp_f32 v130, v130\n"
    "v_mfma_f32_16x16x32_bf16 v[148:151], a[112:115], a[80:83], v[216:219]\n"
    "v_mfma_f32_16x16x32_bf16 v[148:151], a[116:119], a[84:87], v[148:151]\n"
    "v_exp_f32 v131, v131\n"
    "v_mfma_f32_16x16x32_bf16 v[148:151], a[120:123], a[88:91], v[148:151]\n"
    "ds_read_b64_tr_b16 a[160:161], v3 offset:32768\n"
    "v_cvt_pk_bf16_f32 v185, v130, v131\n"
    "v_mfma_f32_16x16x32_bf16 v[148:151], a[124:127], a[92:95], v[148:151]\n"
    "v_exp_f32 v144, v144\n"
    "v_mfma_f32_16x16x32_bf16 v[120:123], a[128:131], a[48:51], v[208:211]\n"
    "ds_read_b64_tr_b16 a[162:163], v3 offset:36864\n"
    "ds_read_b64_tr_b16 a[164:165], v3 offset:40960\n"
    "v_mfma_f32_16x16x32_bf16 v[120:123], a[132:135], a[52:55], v[120:123]\n"
    "v_exp_f32 v145, v145\n"
    "v_mfma_f32_16x16x32_bf16 v[120:123], a[136:139], a[56:59], v[120:123]\n"
    "ds_read_b64_tr_b16 a[166:167], v3 offset:45056\n"
    "v_cvt_pk_bf16_f32 v200, v144, v145\n"
    "v_mfma_f32_16x16x32_bf16 v[120:123], a[140:143], a[60:63], v[120:123]\n"
    "v_exp_f32 v146, v146\n"
    "v_mfma_f32_16x16x32_bf16 v[136:139], a[128:131], a[64:67], v[212:215]\n"
    "ds_read_b64_tr_b16 a[168:169], v4 offset:32768\n"
    "ds_read_b64_tr_b16 a[170:171], v4 offset:36864\n"
    "v_mfma_f32_16x16x32_bf16 v[136:139], a[132:135], a[68:71], v[136:139]\n"
    "v_exp_f32 v147, v147\n"
    "v_mfma_f32_16x16x32_bf16 v[136:139], a[136:139], a[72:75], v[136:139]\n"
    "ds_read_b64_tr_b16 a[172:173], v4 offset:40960\n"
    "v_cvt_pk_bf16_f32 v201, v146, v147\n"
    "v_mfma_f32_16x16x32_bf16 v[136:139], a[140:143], a[76:79], v[136:139]\n"
    "v_exp_f32 v116, v116\n"
    "v_mfma_f32_16x16x32_bf16 v[152:155], a[128:131], a[80:83], v[216:219]\n"
    "ds_read_b64_tr_b16 a[174:175], v4 offset:45056\n"
    "v_mfma_f32_16x16x32_bf16 v[152:155], a[132:135], a[84:87], v[152:155]\n"
    "v_exp_f32 v117, v117\n"
    "v_mfma_f32_16x16x32_bf16 v[152:155], a[136:139], a[88:91], v[152:155]\n"
    "ds_read_b64_tr_b16 a[176:177], v3 offset:33280\n"
    "v_cvt_pk_bf16_f32 v170, v116, v117\n"
    "v_mfma_f32_16x16x32_bf16 v[152:155], a[140:143], a[92:95], v[152:155]\n"
    "v_exp_f32 v118, v118\n"
    "v_mfma_f32_16x16x32_bf16 v[124:127], a[144:147], a[48:51], v[208:211]\n"
    "ds_read_b64_tr_b16 a[178:179], v3 offset:37376\n"
    "ds_read_b64_tr_b16 a[180:181], v3 offset:41472\n"
    "v_mfma_f32_16x16x32_bf16 v[124:127], a[148:151], a[52:55], v[124:127]\n"
    "v_exp_f32 v119, v119\n"
    "v_mfma_f32_16x16x32_bf16 v[124:127], a[152:155], a[56:59], v[124:127]\n"
    "ds_read_b64_tr_b16 a[182:183], v3 offset:45568\n"
    "v_cvt_pk_bf16_f32 v171, v118, v119\n"
    "v_mfma_f32_16x16x32_bf16 v[124:127], a[156:159], a[60:63], v[124:127]\n"
    "v_exp_f32 v132, v132\n"
    "v_mfma_f32_16x16x32_bf16 v[140:143], a[144:147], a[64:67], v[212:215]\n"
    "ds_read_b64_tr_b16 a[184:185], v4 offset:33280\n"
    "ds_read_b64_tr_b16 a[186:187], v4 offset:37376\n"
    "v_mfma_f32_16x16x32_bf16 v[140:143], a[148:151], a[68:71], v[140:143]\n"
    "v_exp_f32 v133, v133\n"
    "v_mfma_f32_16x16x32_bf16 v[140:143], a[152:155], a[72:75], v[140:143]\n"
    "ds_read_b64_tr_b16 a[188:189], v4 offset:41472\n"
    "v_cvt_pk_bf16_f32 v186, v132, v133\n"
    "v_mfma_f32_16x16x32_bf16 v[140:143], a[156:159], a[76:79], v[140:143]\n"
    "v_exp_f32 v134, v134\n"
    "v_mfma_f32_16x16x32_bf16 v[156:159], a[144:147], a[80:83], v[216:219]\n"
    "ds_read_b64_tr_b16 a[190:191], v4 offset:45568\n"
    "v_mfma_f32_16x16x32_bf16 v[156:159], a[148:151], a[84:87], v[156:159]\n"
    "v_exp_f32 v135, v135\n"
    "v_mfma_f32_16x16x32_bf16 v[156:159], a[152:155], a[88:91], v[156:159]\n"
    "ds_read_b64_tr_b16 a[192:193], v3 offset:33792\n"
    "v_cvt_pk_bf16_f32 v187, v134, v135\n"
    "v_mfma_f32_16x16x32_bf16 v[156:159], a[156:159], a[92:95], v[156:159]\n"
    "v_exp_f32 v148, v148\n"
    "v_mfma_f32_16x16x32_bf16 a[224:227], a[236:239], v[160:163], a[224:227]\n"
    "ds_read_b64_tr_b16 a[194:195], v3 offset:37888\n"
    "ds_read_b64_tr_b16 a[196:197], v3 offset:41984\n"
    "v_mfma_f32_16x16x32_bf16 a[224:227], a[236:239], v[164:167], a[224:227]\n"
    "v_exp_f32 v149, v149\n"
    "v_mfma_f32_16x16x32_bf16 a[228:231], a[236:239], v[176:179], a[228:231]\n"
    "ds_read_b64_tr_b16 a[198:199], v3 offset:46080\n"
    "v_cvt_pk_bf16_f32 v202, v148, v149\n"
    "v_mfma_f32_16x16x32_bf16 a[228:231], a[236:239], v[180:183], a[228:231]\n"
    "v_exp_f32 v150, v150\n"
    "v_mfma_f32_16x16x32_bf16 a[232:235], a[236:239], v[192:195], a[232:235]\n"
    "ds_read_b64_tr_b16 a[200:201], v4 offset:33792\n"
    "ds_read_b64_tr_b16 a[202:203], v4 offset:37888\n"
    "v_mfma_f32_16x16x32_bf16 a[232:235], a[236:239], v[196:199], a[232:235]\n"
    "s_waitcnt lgkmcnt(5)\n"
    "v_exp_f32 v151, v151\n"
    "v_mfma_f32_16x16x32_bf16 v[16:19], a[160:163], v[160:163], v[16:19]\n"
    "v_exp_f32 v120, v120\n"
    "v_mfma_f32_16x16x32_bf16 v[16:19], a[164:167], v[164:167], v[16:19]\n"
    "ds_read_b64_tr_b16 a[204:205], v4 offset:41984\n"
    "v_cvt_pk_bf16_f32 v203, v150, v151\n"
    "v_mfma_f32_16x16x32_bf16 v[20:23], a[160:163], v[176:179], v[20:23]\n"
    "v_exp_f32 v121, v121\n"
    "v_mfma_f32_16x16x32_bf16 v[20:23], a[164:167], v[180:183], v[20:23]\n"
    "ds_read_b64_tr_b16 a[206:207], v4 offset:46080\n"
    "v_cvt_pk_bf16_f32 v172, v120, v121\n"
    "v_mfma_f32_16x16x32_bf16 v[24:27], a[160:163], v[192:195], v[24:27]\n"
    "v_exp_f32 v122, v122\n"
    "v_mfma_f32_16x16x32_bf16 v[24:27], a[164:167], v[196:199], v[24:27]\n"
    "ds_read_b64_tr_b16 a[208:209], v3 offset:34304\n"
    "ds_read_b64_tr_b16 a[210:211], v3 offset:38400\n"
    "v_mfma_f32_16x16x32_bf16 v[28:31], a[168:171], v[160:163], v[28:31]\n"
    "v_exp_f32 v123, v123\n"
    "v_mfma_f32_16x16x32_bf16 v[28:31], a[172:175], v[164:167], v[28:31]\n"
    "ds_read_b64_tr_b16 a[212:213], v3 offset:42496\n"
    "ds_read_b64_tr_b16 a[214:215], v3 offset:46592\n"
    "v_mfma_f32_16x16x32_bf16 v[32:35], a[168:171], v[176:179], v[32:35]\n"
    "v_exp_f32 v136, v136\n"
    "v_mfma_f32_16x16x32_bf16 v[32:35], a[172:175], v[180:183], v[32:35]\n"
    "ds_read_b64_tr_b16 a[222:223], v4 offset:46592\n"
    "v_cvt_pk_bf16_f32 v173, v122, v123\n"
    "v_mfma_f32_16x16x32_bf16 v[36:39], a[168:171], v[192:195], v[36:39]\n"
    "v_exp_f32 v137, v137\n"
    "v_mfma_f32_16x16x32_bf16 v[36:39], a[172:175], v[196:199], v[36:39]\n"
    "ds_read_b64_tr_b16 a[216:217], v4 offset:34304\n"
    "ds_read_b64_tr_b16 a[218:219], v4 offset:38400\n"
    "v_mfma_f32_16x16x32_bf16 v[40:43], a[176:179], v[160:163], v[40:43]\n"
    "v_exp_f32 v138, v138\n"
    "v_mfma_f32_16x16x32_bf16 v[40:43], a[180:183], v[164:167], v[40:43]\n"
    "ds_read_b64_tr_b16 a[220:221], v4 offset:42496\n"
    "v_cvt_pk_bf16_f32 v188, v136, v137\n"
    "v_mfma_f32_16x16x32_bf16 v[44:47], a[176:179], v[176:179], v[44:47]\n"
    "v_exp_f32 v139, v139\n"
    "v_mfma_f32_16x16x32_bf16 v[44:47], a[180:183], v[180:183], v[44:47]\n"
    "v_cvt_pk_bf16_f32 v189, v138, v139\n"
    "v_mfma_f32_16x16x32_bf16 v[48:51], a[176:179], v[192:195], v[48:51]\n"
    "v_exp_f32 v152, v152\n"
    "v_mfma_f32_16x16x32_bf16 v[48:51], a[180:183], v[196:199], v[48:51]\n"
    "v_mfma_f32_16x16x32_bf16 v[52:55], a[184:187], v[160:163], v[52:55]\n"
    "v_exp_f32 v153, v153\n"
    "v_mfma_f32_16x16x32_bf16 v[52:55], a[188:191], v[164:167], v[52:55]\n"
    "v_cvt_pk_bf16_f32 v204, v152, v153\n"
    "v_mfma_f32_16x16x32_bf16 v[56:59], a[184:187], v[176:179], v[56:59]\n"
    "v_exp_f32 v154, v154\n"
    "v_mfma_f32_16x16x32_bf16 v[56:59], a[188:191], v[180:183], v[56:59]\n"
    "v_mfma_f32_16x16x32_bf16 v[60:63], a[184:187], v[192:195], v[60:63]\n"
    "v_exp_f32 v155, v155\n"
    "v_mfma_f32_16x16x32_bf16 v[60:63], a[188:191], v[196:199], v[60:63]\n"
    "s_waitcnt lgkmcnt(0)\n"
    "v_cvt_pk_bf16_f32 v205, v154, v155\n"
    "v_mfma_f32_16x16x32_bf16 v[64:67], a[192:195], v[160:163], v[64:67]\n"
    "v_exp_f32 v124, v124\n"
    "v_mfma_f32_16x16x32_bf16 v[64:67], a[196:199], v[164:167], v[64:67]\n"
    "v_mfma_f32_16x16x32_bf16 v[68:71], a[192:195], v[176:179], v[68:71]\n"
    "v_exp_f32 v125, v125\n"
    "v_mfma_f32_16x16x32_bf16 v[68:71], a[196:199], v[180:183], v[68:71]\n"
    "v_cvt_pk_bf16_f32 v174, v124, v125\n"
    "v_mfma_f32_16x16x32_bf16 v[72:75], a[192:195], v[192:195], v[72:75]\n"
    "v_exp_f32 v126, v126\n"
    "v_mfma_f32_16x16x32_bf16 v[72:75], a[196:199], v[196:199], v[72:75]\n"
    "v_mfma_f32_16x16x32_bf16 v[76:79], a[200:203], v[160:163], v[76:79]\n"
    "v_exp_f32 v127, v127\n"
    "v_mfma_f32_16x16x32_bf16 v[76:79], a[204:207], v[164:167], v[76:79]\n"
    "v_cvt_pk_bf16_f32 v175, v126, v127\n"
    "v_mfma_f32_16x16x32_bf16 v[80:83], a[200:203], v[176:179], v[80:83]\n"
    "v_exp_f32 v140, v140\n"
    "v_mfma_f32_16x16x32_bf16 v[80:83], a[204:207], v[180:183], v[80:83]\n"
    "v_mfma_f32_16x16x32_bf16 v[84:87], a[200:203], v[192:195], v[84:87]\n"
    "v_exp_f32 v141, v141\n"
    "v_mfma_f32_16x16x32_bf16 v[84:87], a[204:207], v[196:199], v[84:87]\n"
    "v_cvt_pk_bf16_f32 v190, v140, v141\n"
    "v_mfma_f32_16x16x32_bf16 v[88:91], a[208:211], v[160:163], v[88:91]\n"
    "v_exp_f32 v142, v142\n"
    "v_mfma_f32_16x16x32_bf16 v[88:91], a[212:215], v[164:167], v[88:91]\n"
    "v_mfma_f32_16x16x32_bf16 v[92:95], a[208:211], v[176:179], v[92:95]\n"
    "v_exp_f32 v143, v143\n"
    "v_mfma_f32_16x16x32_bf16 v[92:95], a[212:215], v[180:183], v[92:95]\n"
    "v_cvt_pk_bf16_f32 v191, v142, v143\n"
    "v_mfma_f32_16x16x32_bf16 v[96:99], a[208:211], v[192:195], v[96:99]\n"
    "v_exp_f32 v156, v156\n"
    "v_mfma_f32_16x16x32_bf16 v[96:99], a[212:215], v[196:199], v[96:99]\n"
    "v_mfma_f32_16x16x32_bf16 v[100:103], a[216:219], v[160:163], v[100:103]\n"
    "v_exp_f32 v157, v157\n"
    "v_mfma_f32_16x16x32_bf16 v[100:103], a[220:223], v[164:167], v[100:103]\n"
    "v_cvt_pk_bf16_f32 v206, v156, v157\n"
    "v_mfma_f32_16x16x32_bf16 v[104:107], a[216:219], v[176:179], v[104:107]\n"
    "v_exp_f32 v158, v158\n"
    "v_mfma_f32_16x16x32_bf16 v[104:107], a[220:223], v[180:183], v[104:107]\n"
    "v_mfma_f32_16x16x32_bf16 v[108:111], a[216:219], v[192:195], v[108:111]\n"
    "s_waitcnt vmcnt(0) lgkmcnt(0)\n"
    "v_exp_f32 v159, v159\n"
    "v_mfma_f32_16x16x32_bf16 v[108:111], a[220:223], v[196:199], v[108:111]\n"
    "s_barrier\n"
    "v_cvt_pk_bf16_f32 v207, v158, v159\n"
    "v_mfma_f32_16x16x32_bf16 a[224:227], a[236:239], v[168:171], a[224:227]\n"
    "v_mfma_f32_16x16x32_bf16 a[224:227], a[236:239], v[172:175], a[224:227]\n"
    "ds_read_b64_tr_b16 a[160:161], v3 offset:0\n"
    "ds_read_b64_tr_b16 a[162:163], v3 offset:4096\n"
    "ds_read_b64_tr_b16 a[164:165], v3 offset:8192\n"
    "ds_read_b64_tr_b16 a[166:167], v3 offset:12288\n"
    "ds_read_b64_tr_b16 a[168:169], v4 offset:0\n"
    "ds_read_b64_tr_b16 a[170:171], v4 offset:4096\n"
    "v_mfma_f32_16x16x32_bf16 a[228:231], a[236:239], v[184:187], a[228:231]\n"
    "ds_read_b64_tr_b16 a[172:173], v4 offset:8192\n"
    "ds_read_b64_tr_b16 a[174:175], v4 offset:12288\n"
    "v_mfma_f32_16x16x32_bf16 a[228:231], a[236:239], v[188:191], a[228:231]\n"
    "ds_read_b64_tr_b16 a[176:177], v3 offset:512\n"
    "ds_read_b64_tr_b16 a[178:179], v3 offset:4608\n"
    "v_mfma_f32_16x16x32_bf16 a[232:235], a[236:239], v[200:203], a[232:235]\n"
    "ds_read_b64_tr_b16 a[180:181], v3 offset:8704\n"
    "ds_read_b64_tr_b16 a[182:183], v3 offset:12800\n"
    "v_mfma_f32_16x16x32_bf16 a[232:235], a[236:239], v[204:207], a[232:235]\n"
    "s_waitcnt lgkmcnt(10)\n"
    "ds_read_b64_tr_b16 a[184:185], v4 offset:512\n"
    "ds_read_b64_tr_b16 a[186:187], v4 offset:4608\n"
    "v_mfma_f32_16x16x32_bf16 v[16:19], a[160:163], v[168:171], v[16:19]\n"
    "s_waitcnt lgkmcnt(10)\n"
    "ds_read_b64_tr_b16 a[188:189], v4 offset:8704\n"
    "ds_read_b64_tr_b16 a[190:191], v4 offset:12800\n"
    "v_mfma_f32_16x16x32_bf16 v[16:19], a[164:167], v[172:175], v[16:19]\n"
    "s_waitcnt lgkmcnt(10)\n"
    "ds_read_b64_tr_b16 a[192:193], v3 offset:1024\n"
    "ds_read_b64_tr_b16 a[194:195], v3 offset:5120\n"
    "v_mfma_f32_16x16x32_bf16 v[28:31], a[168:171], v[168:171], v[28:31]\n"
    "s_waitcnt lgkmcnt(10)\n"
    "ds_read_b64_tr_b16 a[196:197], v3 offset:9216\n"
    "ds_read_b64_tr_b16 a[198:199], v3 offset:13312\n"
    "v_mfma_f32_16x16x32_bf16 v[28:31], a[172:175], v[172:175], v[28:31]\n"
    "s_waitcnt lgkmcnt(10)\n"
    "ds_read_b64_tr_b16 a[200:201], v4 offset:1024\n"
    "ds_read_b64_tr_b16 a[202:203], v4 offset:5120\n"
    "v_mfma_f32_16x16x32_bf16 v[40:43], a[176:179], v[168:171], v[40:43]\n"
    "s_waitcnt lgkmcnt(10)\n"
    "ds_read_b64_tr_b16 a[204:205], v4 offset:9216\n"
    "ds_read_b64_tr_b16 a[206:207], v4 offset:13312\n"
    "v_mfma_f32_16x16x32_bf16 v[40:43], a[180:183], v[172:175], v[40:43]\n"
    "s_waitcnt lgkmcnt(10)\n"
    "ds_read_b64_tr_b16 a[208:209], v3 offset:1536\n"
    "ds_read_b64_tr_b16 a[210:211], v3 offset:5632\n"
    "v_mfma_f32_16x16x32_bf16 v[52:55], a[184:187], v[168:171], v[52:55]\n"
    "s_waitcnt lgkmcnt(10)\n"
    "ds_read_b64_tr_b16 a[212:213], v3 offset:9728\n"
    "ds_read_b64_tr_b16 a[214:215], v3 offset:13824\n"
    "v_mfma_f32_16x16x32_bf16 v[52:55], a[188:191], v[172:175], v[52:55]\n"
    "s_waitcnt lgkmcnt(10)\n"
    "ds_read_b64_tr_b16 a[216:217], v4 offset:1536\n"
    "ds_read_b64_tr_b16 a[218:219], v4 offset:5632\n"
    "v_mfma_f32_16x16x32_bf16 v[64:67], a[192:195], v[168:171], v[64:67]\n"
    "s_waitcnt lgkmcnt(10)\n"
    "ds_read_b64_tr_b16 a[220:221], v4 offset:9728\n"
    "ds_read_b64_tr_b16 a[222:223], v4 offset:13824\n"
    "v_mfma_f32_16x16x32_bf16 v[64:67], a[196:199], v[172:175], v[64:67]\n"
    "s_waitcnt lgkmcnt(10)\n"
    "v_accvgpr_read_b32 v247, a224\n"
    "v_cmp_ngt_f32 s[94:95], s70, v247\n"
    "v_mfma_f32_16x16x32_bf16 v[76:79], a[200:203], v[168:171], v[76:79]\n"
    "s_waitcnt lgkmcnt(8)\n"
    "v_rcp_f32 v247, v247\n"
    "v_mfma_f32_16x16x32_bf16 v[76:79], a[204:207], v[172:175], v[76:79]\n"
    "s_waitcnt lgkmcnt(6)\n"
    "v_accvgpr_read_b32 v248, a228\n"
    "v_cmp_ngt_f32 s[96:97], s70, v248\n"
    "v_mul_f32 v16, v247, v16\n"
    "v_mul_f32 v17, v247, v17\n"
    "v_mul_f32 v52, v247, v52\n"
    "v_mfma_f32_16x16x32_bf16 v[88:91], a[208:211], v[168:171], v[88:91]\n"
    "s_waitcnt lgkmcnt(4)\n"
    "v_rcp_f32 v248, v248\n"
    "v_cvt_pk_bf16_f32 v16, v16, v17\n"
    "v_mul_f32 v28, v247, v28\n"
    "v_mul_f32 v55, v247, v55\n"
    "v_mfma_f32_16x16x32_bf16 v[88:91], a[212:215], v[172:175], v[88:91]\n"
    "s_waitcnt lgkmcnt(2)\n"
    "v_accvgpr_read_b32 v249, a232\n"
    "v_cmp_ngt_f32 s[98:99], s70, v249\n"
    "v_mul_f32 v30, v247, v30\n"
    "v_mul_f32 v31, v247, v31\n"
    "v_mfma_f32_16x16x32_bf16 v[100:103], a[216:219], v[168:171], v[100:103]\n"
    "s_waitcnt lgkmcnt(0)\n"
    "v_rcp_f32 v249, v249\n"
    "v_mul_f32 v41, v247, v41\n"
    "v_mul_f32 v42, v247, v42\n"
    "v_mfma_f32_16x16x32_bf16 v[100:103], a[220:223], v[172:175], v[100:103]\n"
    "v_mul_f32 v18, v247, v18\n"
    "v_mul_f32 v19, v247, v19\n"
    "v_mul_f32 v53, v247, v53\n"
    "v_mul_f32 v54, v247, v54\n"
    "v_mfma_f32_16x16x32_bf16 v[20:23], a[160:163], v[184:187], v[20:23]\n"
    "v_cvt_pk_bf16_f32 v17, v18, v19\n"
    "v_mul_f32 v29, v247, v29\n"
    "v_cvt_pk_bf16_f32 v52, v52, v53\n"
    "v_mul_f32 v64, v247, v64\n"
    "v_mfma_f32_16x16x32_bf16 v[20:23], a[164:167], v[188:191], v[20:23]\n"
    "ds_write_b64 v12, v[16:17] offset:0\n"
    "v_cvt_pk_bf16_f32 v28, v28, v29\n"
    "v_mul_f32 v40, v247, v40\n"
    "v_mul_f32 v66, v247, v66\n"
    "v_mul_f32 v67, v247, v67\n"
    "v_mfma_f32_16x16x32_bf16 v[32:35], a[168:171], v[184:187], v[32:35]\n"
    "v_cvt_pk_bf16_f32 v29, v30, v31\n"
    "v_mul_f32 v43, v247, v43\n"
    "v_mul_f32 v77, v247, v77\n"
    "v_mul_f32 v78, v247, v78\n"
    "v_mfma_f32_16x16x32_bf16 v[32:35], a[172:175], v[188:191], v[32:35]\n"
    "ds_write_b64 v13, v[28:29] offset:0\n"
    "v_cvt_pk_bf16_f32 v40, v40, v41\n"
    "v_cvt_pk_bf16_f32 v41, v42, v43\n"
    "v_mul_f32 v88, v247, v88\n"
    "v_mul_f32 v89, v247, v89\n"
    "v_mfma_f32_16x16x32_bf16 v[44:47], a[176:179], v[184:187], v[44:47]\n"
    "ds_write_b64 v14, v[40:41] offset:0\n"
    "v_cvt_pk_bf16_f32 v53, v54, v55\n"
    "v_mul_f32 v65, v247, v65\n"
    "v_mul_f32 v90, v247, v90\n"
    "v_mul_f32 v91, v247, v91\n"
    "v_mfma_f32_16x16x32_bf16 v[44:47], a[180:183], v[188:191], v[44:47]\n"
    "ds_write_b64 v15, v[52:53] offset:0\n"
    "v_cvt_pk_bf16_f32 v64, v64, v65\n"
    "v_mul_f32 v76, v247, v76\n"
    "v_mul_f32 v101, v247, v101\n"
    "v_mul_f32 v102, v247, v102\n"
    "v_mfma_f32_16x16x32_bf16 v[56:59], a[184:187], v[184:187], v[56:59]\n"
    "v_cvt_pk_bf16_f32 v65, v66, v67\n"
    "v_mul_f32 v79, v247, v79\n"
    "v_mul_f32 v20, v248, v20\n"
    "v_mul_f32 v21, v248, v21\n"
    "v_mfma_f32_16x16x32_bf16 v[56:59], a[188:191], v[188:191], v[56:59]\n"
    "ds_write_b64 v12, v[64:65] offset:2048\n"
    "v_cvt_pk_bf16_f32 v76, v76, v77\n"
    "v_cvt_pk_bf16_f32 v77, v78, v79\n"
    "v_mul_f32 v22, v248, v22\n"
    "v_mul_f32 v23, v248, v23\n"
    "v_mfma_f32_16x16x32_bf16 v[68:71], a[192:195], v[184:187], v[68:71]\n"
    "ds_write_b64 v13, v[76:77] offset:2048\n"
    "v_cvt_pk_bf16_f32 v88, v88, v89\n"
    "v_mul_f32 v100, v247, v100\n"
    "v_mul_f32 v33, v248, v33\n"
    "v_mul_f32 v34, v248, v34\n"
    "v_mfma_f32_16x16x32_bf16 v[68:71], a[196:199], v[188:191], v[68:71]\n"
    "ds_read_b128 v[232:235], v10 offset:0\n"
    "v_cvt_pk_bf16_f32 v89, v90, v91\n"
    "v_mul_f32 v103, v247, v103\n"
    "v_mul_f32 v44, v248, v44\n"
    "v_mfma_f32_16x16x32_bf16 v[80:83], a[200:203], v[184:187], v[80:83]\n"
    "v_cvt_pk_bf16_f32 v100, v100, v101\n"
    "v_cvt_pk_bf16_f32 v101, v102, v103\n"
    "v_mul_f32 v45, v248, v45\n"
    "v_mul_f32 v46, v248, v46\n"
    "v_mfma_f32_16x16x32_bf16 v[80:83], a[204:207], v[188:191], v[80:83]\n"
    "ds_read_b128 v[236:239], v10 offset:1024\n"
    "v_cvt_pk_bf16_f32 v20, v20, v21\n"
    "v_mul_f32 v32, v248, v32\n"
    "v_mul_f32 v56, v248, v56\n"
    "v_mfma_f32_16x16x32_bf16 v[92:95], a[208:211], v[184:187], v[92:95]\n"
    "v_cvt_pk_bf16_f32 v21, v22, v23\n"
    "v_mul_f32 v35, v248, v35\n"
    "v_mul_f32 v58, v248, v58\n"
    "v_mul_f32 v59, v248, v59\n"
    "v_mfma_f32_16x16x32_bf16 v[92:95], a[212:215], v[188:191], v[92:95]\n"
    "ds_write_b64 v14, v[88:89] offset:2048\n"
    "v_cvt_pk_bf16_f32 v32, v32, v33\n"
    "v_cvt_pk_bf16_f32 v33, v34, v35\n"
    "v_mfma_f32_16x16x32_bf16 v[104:107], a[216:219], v[184:187], v[104:107]\n"
    "ds_write_b64 v12, v[20:21] offset:4096\n"
    "v_mul_f32 v47, v248, v47\n"
    "v_cvt_pk_bf16_f32 v44, v44, v45\n"
    "v_mul_f32 v70, v248, v70\n"
    "v_mul_f32 v71, v248, v71\n"
    "v_mfma_f32_16x16x32_bf16 v[104:107], a[220:223], v[188:191], v[104:107]\n"
    "ds_write_b64 v15, v[100:101] offset:2048\n"
    "v_cvt_pk_bf16_f32 v45, v46, v47\n"
    "v_mul_f32 v57, v248, v57\n"
    "v_mfma_f32_16x16x32_bf16 v[24:27], a[160:163], v[200:203], v[24:27]\n"
    "v_cvt_pk_bf16_f32 v56, v56, v57\n"
    "v_mul_f32 v68, v248, v68\n"
    "v_mul_f32 v82, v248, v82\n"
    "v_mul_f32 v83, v248, v83\n"
    "v_mfma_f32_16x16x32_bf16 v[24:27], a[164:167], v[204:207], v[24:27]\n"
    "s_waitcnt lgkmcnt(4)\n"
    "buffer_store_dwordx4 v[232:235], v11, s[52:55], s75 offen nt\n"
    "ds_read_b128 v[220:223], v10 offset:3072\n"
    "v_cvt_pk_bf16_f32 v57, v58, v59\n"
    "v_mul_f32 v69, v248, v69\n"
    "v_mfma_f32_16x16x32_bf16 v[36:39], a[168:171], v[200:203], v[36:39]\n"
    "v_cvt_pk_bf16_f32 v68, v68, v69\n"
    "v_mul_f32 v80, v248, v80\n"
    "v_mul_f32 v94, v248, v94\n"
    "v_mul_f32 v95, v248, v95\n"
    "v_mfma_f32_16x16x32_bf16 v[36:39], a[172:175], v[204:207], v[36:39]\n"
    "s_waitcnt lgkmcnt(4)\n"
    "ds_read_b128 v[240:243], v10 offset:2048\n"
    "buffer_store_dwordx4 v[236:239], v11, s[52:55], s75 offen offset:2048 nt\n"
    "v_cvt_pk_bf16_f32 v69, v70, v71\n"
    "v_mul_f32 v81, v248, v81\n"
    "v_mfma_f32_16x16x32_bf16 v[48:51], a[176:179], v[200:203], v[48:51]\n"
    "v_cvt_pk_bf16_f32 v80, v80, v81\n"
    "v_mul_f32 v92, v248, v92\n"
    "v_mul_f32 v106, v248, v106\n"
    "v_mul_f32 v107, v248, v107\n"
    "v_mfma_f32_16x16x32_bf16 v[48:51], a[180:183], v[204:207], v[48:51]\n"
    "ds_write_b64 v13, v[32:33] offset:4096\n"
    "v_cvt_pk_bf16_f32 v81, v82, v83\n"
    "v_mul_f32 v93, v248, v93\n"
    "v_mfma_f32_16x16x32_bf16 v[60:63], a[184:187], v[200:203], v[60:63]\n"
    "ds_write_b64 v12, v[68:69] offset:6144\n"
    "v_cvt_pk_bf16_f32 v92, v92, v93\n"
    "v_mul_f32 v104, v248, v104\n"
    "v_mul_f32 v26, v249, v26\n"
    "v_mul_f32 v27, v249, v27\n"
    "v_mfma_f32_16x16x32_bf16 v[60:63], a[188:191], v[204:207], v[60:63]\n"
    "ds_write_b64 v14, v[44:45] offset:4096\n"
    "v_cvt_pk_bf16_f32 v93, v94, v95\n"
    "v_mul_f32 v105, v248, v105\n"
    "v_mfma_f32_16x16x32_bf16 v[72:75], a[192:195], v[200:203], v[72:75]\n"
    "ds_write_b64 v13, v[80:81] offset:6144\n"
    "v_cvt_pk_bf16_f32 v104, v104, v105\n"
    "v_mul_f32 v24, v249, v24\n"
    "v_mul_f32 v38, v249, v38\n"
    "v_mul_f32 v39, v249, v39\n"
    "v_mfma_f32_16x16x32_bf16 v[72:75], a[196:199], v[204:207], v[72:75]\n"
    "ds_write_b64 v15, v[56:57] offset:4096\n"
    "v_cvt_pk_bf16_f32 v105, v106, v107\n"
    "v_mul_f32 v25, v249, v25\n"
    "v_mfma_f32_16x16x32_bf16 v[84:87], a[200:203], v[200:203], v[84:87]\n"
    "v_cvt_pk_bf16_f32 v24, v24, v25\n"
    "v_mul_f32 v36, v249, v36\n"
    "v_mul_f32 v50, v249, v50\n"
    "v_mul_f32 v51, v249, v51\n"
    "v_mfma_f32_16x16x32_bf16 v[84:87], a[204:207], v[204:207], v[84:87]\n"
    "s_waitcnt lgkmcnt(5)\n"
    "buffer_store_dwordx4 v[240:243], v11, s[52:55], s75 offen offset:128 nt\n"
    "ds_read_b128 v[224:227], v10 offset:4096\n"
    "v_cvt_pk_bf16_f32 v25, v26, v27\n"
    "v_mul_f32 v37, v249, v37\n"
    "v_mfma_f32_16x16x32_bf16 v[96:99], a[208:211], v[200:203], v[96:99]\n"
    "Lpg8%=:\n"
    "s_cbranch_scc1 Lback%=\n"
    "buffer_store_dwordx4 v[220:223], v11, s[52:55], s75 offen offset:2176 nt\n"
    "v_cvt_pk_bf16_f32 v36, v36, v37\n"
    "v_mul_f32 v48, v249, v48\n"
    "v_mul_f32 v62, v249, v62\n"
    "v_mul_f32 v63, v249, v63\n"
    "v_mfma_f32_16x16x32_bf16 v[96:99], a[212:215], v[204:207], v[96:99]\n"
    "ds_read_b128 v[228:231], v10 offset:5120\n"
    "v_cvt_pk_bf16_f32 v37, v38, v39\n"
    "v_mul_f32 v49, v249, v49\n"
    "v_mul_f32 v72, v249, v72\n"
    "v_mul_f32 v75, v249, v75\n"
    "v_mfma_f32_16x16x32_bf16 v[108:111], a[216:219], v[200:203], v[108:111]\n"
    "ds_write_b64 v14, v[92:93] offset:6144\n"
    "ds_write_b64 v12, v[24:25] offset:8192\n"
    "v_cvt_pk_bf16_f32 v48, v48, v49\n"
    "v_mul_f32 v60, v249, v60\n"
    "v_mul_f32 v73, v249, v73\n"
    "v_mul_f32 v74, v249, v74\n"
    "v_mfma_f32_16x16x32_bf16 v[108:111], a[220:223], v[204:207], v[108:111]\n"
    "ds_write_b64 v15, v[104:105] offset:6144\n"
    "ds_write_b64 v13, v[36:37] offset:8192\n"
    "v_cvt_pk_bf16_f32 v49, v50, v51\n"
    "v_mul_f32 v61, v249, v61\n"
    "v_cvt_pk_bf16_f32 v60, v60, v61\n"
    "v_cvt_pk_bf16_f32 v61, v62, v63\n"
    "v_cvt_pk_bf16_f32 v72, v72, v73\n"
    "v_mul_f32 v84, v249, v84\n"
    "v_cvt_pk_bf16_f32 v73, v74, v75\n"
    "v_mul_f32 v85, v249, v85\n"
    "v_mul_f32 v86, v249, v86\n"
    "v_mul_f32 v87, v249, v87\n"
    "v_cvt_pk_bf16_f32 v84, v84, v85\n"
    "v_cvt_pk_bf16_f32 v85, v86, v87\n"
    "ds_write_b64 v14, v[48:49] offset:8192\n"
    "ds_write_b64 v15, v[60:61] offset:8192\n"
    "ds_write_b64 v12, v[72:73] offset:10240\n"
    "ds_write_b64 v13, v[84:85] offset:10240\n"
    "s_waitcnt lgkmcnt(9)\n"
    "buffer_store_dwordx4 v[224:227], v11, s[52:55], s76 offen nt\n"
    "ds_read_b128 v[232:235], v10 offset:6144\n"
    "ds_read_b128 v[236:239], v10 offset:7168\n"
    "v_mul_f32 v96, v249, v96\n"
    "v_mul_f32 v97, v249, v97\n"
    "v_mul_f32 v98, v249, v98\n"
    "v_mul_f32 v99, v249, v99\n"
    "v_cvt_pk_bf16_f32 v96, v96, v97\n"
    "v_cvt_pk_bf16_f32 v97, v98, v99\n"
    "ds_read_b128 v[240:243], v10 offset:8192\n"
    "ds_read_b128 v[220:223], v10 offset:9216\n"
    "ds_write_b64 v14, v[96:97] offset:10240\n"
    "s_waitcnt lgkmcnt(13)\n"
    "buffer_store_dwordx4 v[228:231], v11, s[52:55], s76 offen offset:2048 nt\n"
    "v_mul_f32 v108, v249, v108\n"
    "v_mul_f32 v109, v249, v109\n"
    "v_mul_f32 v110, v249, v110\n"
    "v_mul_f32 v111, v249, v111\n"
    "v_cvt_pk_bf16_f32 v108, v108, v109\n"
    "v_cvt_pk_bf16_f32 v109, v110, v111\n"
    "ds_write_b64 v15, v[108:109] offset:10240\n"
    "ds_read_b128 v[224:227], v10 offset:10240\n"
    "ds_read_b128 v[228:231], v10 offset:11264\n"
    "s_waitcnt lgkmcnt(7)\n"
    "buffer_store_dwordx4 v[232:235], v11, s[52:55], s76 offen offset:128 nt\n"
    "s_waitcnt lgkmcnt(6)\n"
    "buffer_store_dwordx4 v[236:239], v11, s[52:55], s76 offen offset:2176 nt\n"
    "s_waitcnt lgkmcnt(5)\n"
    "buffer_store_dwordx4 v[240:243], v11, s[52:55], s77 offen nt\n"
    "s_waitcnt lgkmcnt(4)\n"
    "buffer_store_dwordx4 v[220:223], v11, s[52:55], s77 offen offset:2048 nt\n"
    "s_waitcnt lgkmcnt(1)\n"
    "buffer_store_dwordx4 v[224:227], v11, s[52:55], s77 offen offset:128 nt\n"
    "s_waitcnt lgkmcnt(0)\n"
    "buffer_store_dwordx4 v[228:231], v11, s[52:55], s77 offen offset:2176 nt\n"
    "s_or_b64 s[68:69], s[68:69], s[88:89]\n"
    "s_or_b64 s[68:69], s[68:69], s[90:91]\n"
    "s_or_b64 s[68:69], s[68:69], s[92:93]\n"
    "s_or_b64 s[68:69], s[68:69], s[94:95]\n"
    "s_or_b64 s[68:69], s[68:69], s[96:97]\n"
    "s_or_b64 s[68:69], s[68:69], s[98:99]\n"
    "s_mov_b64 %[flag], s[68:69]\n"
    "s_mov_b32 %[wid], s71\n"
    "Lend%=:\n"
      : [tid] "+v"(tid), [flag] "=&s"(flag), [wid] "=&s"(wid)
      : [q] "s"(qh), [k] "s"(kh), [v] "s"(vh), [o] "s"(oh), [lds] "s"(ldsb), [bid] "s"(bid)
      : "v1","v2","v3","v4","v5","v6","v7","v8","v9","v10","v11","v12","v13","v14","v15","v16","v17","v18","v19","v20","v21","v22","v23","v24","v25","v26","v27","v28","v29","v30","v31","v32","v33","v34","v35","v36","v37","v38","v39","v40","v41","v42","v43","v44","v45","v46","v47","v48","v49","v50","v51","v52","v53","v54","v55","v56","v57","v58","v59","v60","v61","v62","v63","v64","v65","v66","v67","v68","v69","v70","v71","v72","v73","v74","v75","v76","v77","v78","v79","v80","v81","v82","v83","v84","v85","v86","v87","v88","v89","v90","v91","v92","v93","v94","v95","v96","v97","v98","v99","v100","v101","v102","v103","v104","v105","v106","v107","v108","v109","v110","v111","v112","v113","v114","v115","v116","v117","v118","v119","v120","v121","v122","v123","v124","v125","v126","v127","v128","v129","v130","v131","v132","v133","v134","v135","v136","v137","v138","v139","v140","v141","v142","v143","v144","v145","v146","v147","v148","v149","v150","v151","v152","v153","v154","v155","v156","v157","v158","v159","v160","v161","v162","v163","v164","v165","v166","v167","v168","v169","v170","v171","v172","v173","v174","v175","v176","v177","v178","v179","v180","v181","v182","v183","v184","v185","v186","v187","v188","v189","v190","v191","v192","v193","v194","v195","v196","v197","v198","v199","v200","v201","v202","v203","v204","v205","v206","v207","v208","v209","v210","v211","v212","v213","v214","v215","v216","v217","v218","v219","v220","v221","v222","v223","v224","v225","v226","v227","v228","v229","v230","v231","v232","v233","v234","v235","v236","v237","v238","v239","v240","v241","v242","v243","v244","v245","v246","v247","v248","v249","v250","v251","v252","v253","v254","v255","a0","a1","a2","a3","a4","a5","a6","a7","a8","a9","a10","a11","a12","a13","a14","a15","a16","a17","a18","a19","a20","a21","a22","a23","a24","a25","a26","a27","a28","a29","a30","a31","a32","a33","a34","a35","a36","a37","a38","a39","a40","a41","a42","a43","a44","a45","a46","a47","a48","a49","a50","a51","a52","a53","a54","a55","a56","a57","a58","a59","a60","a61","a62","a63","a64","a65","a66","a67","a68","a69","a70","a71","a72","a73","a74","a75","a76","a77","a78","a79","a80","a81","a82","a83","a84","a85","a86","a87","a88","a89","a90","a91","a92","a93","a94","a95","a96","a97","a98","a99","a100","a101","a102","a103","a104","a105","a106","a107","a108","a109","a110","a111","a112","a113","a114","a115","a116","a117","a118","a119","a120","a121","a122","a123","a124","a125","a126","a127","a128","a129","a130","a131","a132","a133","a134","a135","a136","a137","a138","a139","a140","a141","a142","a143","a144","a145","a146","a147","a148","a149","a150","a151","a152","a153","a154","a155","a156","a157","a158","a159","a160","a161","a162","a163","a164","a165","a166","a167","a168","a169","a170","a171","a172","a173","a174","a175","a176","a177","a178","a179","a180","a181","a182","a183","a184","a185","a186","a187","a188","a189","a190","a191","a192","a193","a194","a195","a196","a197","a198","a199","a200","a201","a202","a203","a204","a205","a206","a207","a208","a209","a210","a211","a212","a213","a214","a215","a216","a217","a218","a219","a220","a221","a222","a223","a224","a225","a226","a227","a228","a229","a230","a231","a232","a233","a234","a235","a236","a237","a238","a239","a240","a241","a242","a243","a244","a245","a246","a247","a248","a249","a250","a251","a252","a253","a254","a255","s40","s41","s42","s43","s44","s45","s46","s47","s48","s49","s50","s51","s52","s53","s54","s55","s56","s57","s58","s59","s60","s61","s62","s63","s64","s65","s66","s67","s68","s69","s70","s71","s72","s73","s74","s75","s76","s77","s78","s79","s80","s81","s82","s83","s84","s85","s86","s87","s88","s89","s90","s91","s92","s93","s94","s95","s96","s97","s98","s99","vcc","scc","memory");
  if (flag != 0ull) {
    asm volatile("s_waitcnt vmcnt(0)" ::: "memory");
    slow_rows(qh, kh, vh, oh, (int)wid, (int)__builtin_amdgcn_mbcnt_hi(~0u, __builtin_amdgcn_mbcnt_lo(~0u, 0u)));
  }
}

extern "C" void kernel_launch(void* const* d_in, const int* in_sizes, int n_in, void* d_out, int out_size, void* d_ws,
                              size_t ws_size, hipStream_t stream) {
  gqa_attn_fwd<<<256, 256, 0, stream>>>((const unsigned short*)d_in[0], (const unsigned short*)d_in[1],
                                        (const unsigned short*)d_in[2], (unsigned short*)d_out);
}
```
